```python
import torch

def _softmax(x, axis=-1):
    return torch.softmax(x, dim=axis)

def _gelu(x):
    return torch.nn.functional.gelu(x, approximate='tanh')

def _silu(x):
    return torch.nn.functional.silu(x)

def _sigmoid(x):
    return torch.sigmoid(x)

def _layernorm(x, axis=-1, eps=1e-05):
    m = x.mean(dim=axis, keepdim=True)
    v = x.var(dim=axis, keepdim=True, unbiased=False)
    return (x - m) / torch.sqrt(v + eps)

def _scatter_add(tgt, idx, src, axis=0):
    return tgt.clone().index_add_(axis, idx, src)

def _segment_sum(src, seg, n):
    out = torch.zeros((n,) + src.shape[1:], dtype=src.dtype)
    return out.index_add_(0, seg, src)

def _pad(x, pw):
    flat = tuple((v for pair in reversed(pw) for v in pair))
    return torch.nn.functional.pad(x, flat)
kernel_name = 'concat_heads__sliding_local_dilated__memory_aug_recurrent_mem_tr__prompt'

def setup_inputs():

    _w = lambda a: (lambda t: t.to(torch.bfloat16) if t.is_floating_point() else t)(torch.as_tensor(a))

    def _A_setup():
        B, S, D = (1, 2048, 640)
        return {'x': _w((lambda *s: torch.randn(*s, dtype=torch.bfloat16) * 0.2)(B, S, D)), 'wqkv': _w((lambda *s: torch.randn(*s, dtype=torch.bfloat16) * 0.2)(D, 3 * D)), 'wo': _w((lambda *s: torch.randn(*s, dtype=torch.bfloat16) * 0.05)(D, D))}
    _A_in = _A_setup()

    def _B_setup():
        B, S, Dm = (1, 2048, 640)
        H, Dh = (32, 192)
        M, seg = (8, 32)
        seg = min(seg, S)
        S = S // seg * seg
        return {'x': _w((lambda *s: torch.randn(*s, dtype=torch.bfloat16) * 0.05)(B, S, Dm)), 'mem0': _w((lambda *s: torch.randn(*s, dtype=torch.bfloat16) * 1.6)(B, M, Dm)), 'Wq': _w((lambda *s: torch.randn(*s, dtype=torch.bfloat16) * 0.8)(Dm, H, Dh) * Dm ** (-0.5)), 'Wk': _w((lambda *s: torch.randn(*s, dtype=torch.bfloat16) * 0.8)(Dm, H, Dh) * Dm ** (-0.5)), 'Wv': _w((lambda *s: torch.randn(*s, dtype=torch.bfloat16) * 1.6)(Dm, H, Dh) * Dm ** (-0.5)), 'Wo': _w((lambda *s: torch.randn(*s, dtype=torch.bfloat16) * 1.6)(H, Dh, Dm) * (H * Dh) ** (-0.5)), 'seg_len': seg}
    _B_in = _B_setup()
    return {'A_x': _A_in['x'], 'A_wqkv': _A_in['wqkv'], 'A_wo': _A_in['wo'], 'B_x': _B_in['x'], 'B_mem0': _B_in['mem0'], 'B_Wq': _B_in['Wq'], 'B_Wk': _B_in['Wk'], 'B_Wv': _B_in['Wv'], 'B_Wo': _B_in['Wo'], 'B_seg_len': _B_in['seg_len']}

def reference(A_x, A_wqkv, A_wo, B_x, B_mem0, B_Wq, B_Wk, B_Wv, B_Wo, B_seg_len):

    def _A_ref(x, wqkv, wo):
        B, S, D = x.shape
        H = 32
        Dh = D // H
        W, dil = (8, 2)
        qkv = (lambda x, *d: x.permute(*d))((x @ wqkv).reshape(B, S, 3, H, Dh), 2, 0, 3, 1, 4)
        q, k, v = (qkv[0], qkv[1], qkv[2])
        scale = Dh ** (-0.5)
        pos = torch.arange(S)
        off = torch.arange(W) * dil
        idx = pos[:, None] - off[None, :]
        mask = idx >= 0
        idx_c = torch.where(mask, idx, 0).reshape(-1)
        kg = k[:, :, idx_c, :].reshape(B, H, S, W, Dh)
        vg = v[:, :, idx_c, :].reshape(B, H, S, W, Dh)
        logits = torch.einsum('bhsd,bhswd->bhsw', q, kg) * scale
        logits = torch.where(mask[None, None, :, :], logits, -1000000000.0)
        attn = _softmax(logits, axis=-1)
        o = torch.einsum('bhsw,bhswd->bhsd', attn, vg)
        return (lambda x, *d: x.permute(*d))(o, 0, 2, 1, 3).reshape(B, S, D) @ wo

    def _B_ref(x, mem0, Wq, Wk, Wv, Wo, seg_len):
        B, S, Dm = x.shape
        H, Dh = (Wq.shape[1], Wq.shape[2])
        M = mem0.shape[1]
        scale = Dh ** (-0.5)
        n_seg = S // seg_len
        L = M + seg_len
        idx = torch.arange(L)
        mask = (idx[:, None] < M) | (idx[:, None] >= idx[None, :])
        mem = mem0
        outs = []
        for s in range(n_seg):
            xs = x[:, s * seg_len:(s + 1) * seg_len, :].clone()
            h = torch.concatenate([mem, xs], axis=1)
            q = torch.einsum('btd,dhe->bhte', h, Wq)
            k = torch.einsum('btd,dhe->bhte', h, Wk)
            v = torch.einsum('btd,dhe->bhte', h, Wv)
            scores = torch.einsum('bhqe,bhke->bhqk', q, k) * scale
            scores = torch.where(mask[None, None], scores, -1000000000.0)
            p = _softmax(scores, axis=-1)
            o = torch.einsum('bhqk,bhke->bhqe', p, v)
            o = torch.einsum('bhte,hed->btd', o, Wo)
            mem = o[:, :M, :].clone()
            outs.append(o[:, M:, :])
        return torch.concatenate(outs, axis=1)
    a = _A_ref(A_x, A_wqkv, A_wo)
    b = _B_ref(B_x, B_mem0, B_Wq, B_Wk, B_Wv, B_Wo, B_seg_len)
    return torch.concatenate([a, b], axis=-1)
```

```cpp
#include <hip/hip_runtime.h>
#include <hip/hip_bf16.h>
#include <stdint.h>

typedef unsigned short u16;
typedef unsigned long long u64;
typedef __attribute__((ext_vector_type(4))) float f32x4;
typedef __attribute__((address_space(1))) u64 gu64;
typedef __attribute__((address_space(1))) unsigned gu32;
typedef unsigned u32x8 __attribute__((ext_vector_type(8)));
typedef unsigned u32x4 __attribute__((ext_vector_type(4)));
typedef unsigned u32x2 __attribute__((ext_vector_type(2)));
typedef __attribute__((ext_vector_type(4))) short bf16x4;
typedef __attribute__((ext_vector_type(8))) __bf16 bfv8;
typedef __attribute__((ext_vector_type(16))) __bf16 bfv16;
typedef __bf16 bf16x2_t __attribute__((ext_vector_type(2)));
typedef float f32x2_t __attribute__((ext_vector_type(2)));
typedef unsigned v2u_t __attribute__((ext_vector_type(2)));
#define RLX_AGENT __ATOMIC_RELAXED, __HIP_MEMORY_SCOPE_AGENT
#define LAS __attribute__((address_space(3)))

__device__ __forceinline__ float bf2f(u16 v) { return __uint_as_float(((unsigned)v) << 16); }
__device__ __forceinline__ u16 f2bf(float f) { return __builtin_bit_cast(u16, (__bf16)f); }
__device__ __forceinline__ float rbf(float f) { return bf2f(f2bf(f)); }
__device__ __forceinline__ unsigned cvtpk(float lo, float hi) { f32x2_t v = {lo, hi}; return __builtin_bit_cast(unsigned, __builtin_convertvector(v, bf16x2_t)); }
__device__ __forceinline__ __amdgpu_buffer_rsrc_t mkrs(const void* p) { return __builtin_amdgcn_make_buffer_rsrc((void*)p, 0, 0x7fffffff, 0x00020000); }
__device__ __forceinline__ u32x4 ld16(__amdgpu_buffer_rsrc_t rs, unsigned voff, unsigned soff) { return __builtin_amdgcn_raw_buffer_load_b128(rs, (int)voff, (int)soff, 16); }
__device__ __forceinline__ u32x2 ld8(__amdgpu_buffer_rsrc_t rs, unsigned voff, unsigned soff) { return __builtin_amdgcn_raw_buffer_load_b64(rs, (int)voff, (int)soff, 16); }
__device__ __forceinline__ void st16(__amdgpu_buffer_rsrc_t rs, unsigned voff, unsigned soff, u32x4 w) { __builtin_amdgcn_raw_buffer_store_b128(w, rs, (int)voff, (int)soff, 16); }
__device__ __forceinline__ void st8(__amdgpu_buffer_rsrc_t rs, unsigned voff, unsigned soff, unsigned lo, unsigned hi) { u32x2 w = {lo, hi}; __builtin_amdgcn_raw_buffer_store_b64(w, rs, (int)voff, (int)soff, 16); }
__device__ __forceinline__ float max16(float x) { v2u_t r = __builtin_amdgcn_permlane16_swap(__float_as_uint(x), __float_as_uint(x), false, false); return fmaxf(__uint_as_float(r[0]), __uint_as_float(r[1])); }
__device__ __forceinline__ float max32(float x) { v2u_t r = __builtin_amdgcn_permlane32_swap(__float_as_uint(x), __float_as_uint(x), false, false); return fmaxf(__uint_as_float(r[0]), __uint_as_float(r[1])); }
__device__ __forceinline__ float sum16(float x) { v2u_t r = __builtin_amdgcn_permlane16_swap(__float_as_uint(x), __float_as_uint(x), false, false); return __uint_as_float(r[0]) + __uint_as_float(r[1]); }
__device__ __forceinline__ float sum32(float x) { v2u_t r = __builtin_amdgcn_permlane32_swap(__float_as_uint(x), __float_as_uint(x), false, false); return __uint_as_float(r[0]) + __uint_as_float(r[1]); }
__device__ __forceinline__ float tred32(float a, float b) { v2u_t r = __builtin_amdgcn_permlane32_swap(__float_as_uint(a), __float_as_uint(b), false, false); return __uint_as_float(r[0]) + __uint_as_float(r[1]); }
__device__ __forceinline__ unsigned half_of(const char* base, unsigned off, unsigned side, unsigned pol) {
  return off + ((((unsigned)(((size_t)base + off) >> 21) ^ side ^ pol) & 1u) << 13);
}
__device__ __forceinline__ unsigned side_map(const void* base, unsigned L, unsigned side, unsigned pol) { return half_of((const char*)base, (L >> 13) << 14, side, pol) + (L & 8191u); }
constexpr unsigned CH_SPIN_CAP = 1u << 18;
__device__ __forceinline__ bool spin_check(unsigned& spins, unsigned* tmop) {
  asm volatile("" ::: "memory");
  ++spins;
  if ((spins & 63u) == 0u) {
    gu32* tmo = (gu32*)tmop;
    if (__builtin_amdgcn_readfirstlane(__hip_atomic_load(tmo, RLX_AGENT))) return false;
    if (spins > CH_SPIN_CAP) { __hip_atomic_store(tmo, 1u, RLX_AGENT); return false; }
  }
  return true;
}
__device__ __forceinline__ unsigned xcc_side() { return ((unsigned)__builtin_amdgcn_s_getreg((3 << 11) | 20) >> 2) & 1u; }
__device__ __forceinline__ f32x4 mfma(u32x4 a, u32x4 b, f32x4 c) { return __builtin_amdgcn_mfma_f32_16x16x32_bf16(__builtin_bit_cast(bfv8, a), __builtin_bit_cast(bfv8, b), c, 0, 0, 0); }
__device__ __forceinline__ f32x4 smf(u32x4 a, u32x8 b, f32x4 c, int ix) { return __builtin_amdgcn_smfmac_f32_16x16x64_bf16(__builtin_bit_cast(bfv8, a), __builtin_bit_cast(bfv16, b), c, ix, 0, 0); }
__device__ __forceinline__ u32x4 lo4(u32x8 v) { return __builtin_shufflevector(v, v, 0, 1, 2, 3); }
__device__ __forceinline__ u32x4 hi4(u32x8 v) { return __builtin_shufflevector(v, v, 4, 5, 6, 7); }
__device__ __forceinline__ u32x8 cat8(u32x4 a, u32x4 b) { return __builtin_shufflevector(a, b, 0, 1, 2, 3, 4, 5, 6, 7); }
#define BAR() do { asm volatile("s_waitcnt lgkmcnt(0)" ::: "memory"); __builtin_amdgcn_s_barrier(); asm volatile("" ::: "memory"); } while (0)
#define SB() __builtin_amdgcn_sched_barrier(0)
#define VMW(n) asm volatile("s_waitcnt vmcnt(" #n ")" ::: "memory")
#define ZERO4 ((f32x4){0.f, 0.f, 0.f, 0.f})
__device__ __forceinline__ u32x2 trrd(const unsigned char* lds, unsigned off) { return __builtin_bit_cast(u32x2, __builtin_amdgcn_ds_read_tr16_b64_v4i16((LAS bf16x4*)(lds + off))); }
__device__ __forceinline__ u32x4 trrd2(const unsigned char* lds, unsigned off, unsigned d) { const u32x2 lo = trrd(lds, off), hi = trrd(lds, off + d); return (u32x4){lo.x, lo.y, hi.x, hi.y}; }

#define TSK 8
#define APN 2
#define APU 1
#define GFW 6
#define APB 10
#define OPH 3
#define RSELF 3
#define RN3 20
#define XSL 0
#define NU1 4
#define AQ0 2
#define AU0 3
#define LAY_S 16, 0, 16, 0
#define LAY_A 4, 4, 4, 4
#define DUR0 132u
#define BKO 2
#define RFW 4
#define CPF 116000
#define FD0 6
#define CALN 5
#define RLAG 4
#define RN1 7
#define RN2 9
#define DDV 68, 74, 8, 8, 0, 2, 1, 88
#define FSL 4
struct Args {
  const u16* mem0; const u16* Wq; const u16* Wk; const u16* Wv; const u16* Wo; const u16* Bx; const u16* Ax; const u16* Awqkv; const u16* Awo;
  u16* out; char* ws;
  int d[8];
};
constexpr size_t MB = 1u << 20;
constexpr size_t O_CNT = 1 * MB, O_CAL = O_CNT + 65536, O_X1 = 2 * MB;
constexpr unsigned X1_STEP = 32768;
constexpr size_t O_FF = 4 * MB, O_KF = O_FF + 25 * MB, O_QF = O_KF + 24 * MB, O_KM = O_QF + 24 * MB, O_PT = O_KM + 7 * MB, O_UO = O_PT + 8 * MB,
                 O_PS = O_UO + 96 * MB, O_OA = O_PS + 8 * MB, O_END = O_OA + 3 * MB;
static_assert(O_X1 + 64 * 32768 <= O_FF, "");
#define PT_BLK(g_, c_) ((((g_) * ((g_) + 3) / 2 + ((c_) == 4 ? 0 : (c_) + 1)) >> 1) * 16384 + (((g_) * ((g_) + 3) / 2 + ((c_) == 4 ? 0 : (c_) + 1)) & 1) * 4096)
constexpr unsigned PT_SEG = 131072;
constexpr unsigned QK_H = 64 * 12288, QK_S = 12288, U_U = 64 * 16384, U_S = 16384, KM_H = 64 * 3072, KM_S = 3072;
#define CNT(i) (32 * (i))
constexpr int PRE = 8;
#define PSLOT(s_) (((s_) >> 5) * (4 << 20) + ((s_) & 31) * 65536)
constexpr int C_TMO = 8, C_POL = 9, C_FILL = 10, C_AO = 16, C_PT_ = 32, C_PS = 64, C_PO = 96, C_TOK = 192, C_FF = 448, C_TOTAL = 480;
__device__ __forceinline__ float nopk(float x) { asm("" : "+v"(x)); return x; }
__device__ __forceinline__ float fmx(float a, float b) { float big = __builtin_inff(); asm("" : "+s"(big)); return __builtin_amdgcn_fmed3f(a, b, big); }
__device__ __forceinline__ float t32(float a, float b) { return nopk(tred32(a, b)); }
__device__ __forceinline__ int lane_id() { int l = (int)__builtin_amdgcn_mbcnt_hi(~0u, __builtin_amdgcn_mbcnt_lo(~0u, 0u)); asm volatile("" : "+v"(l)); return l; }
#define RT_MARK(t_) asm volatile("s_memrealtime %0\n s_waitcnt lgkmcnt(0)" : "=s"(t_) :: "memory")
__device__ __forceinline__ unsigned wait_rt(unsigned target) {
  int r;
#define RT_S "s_memrealtime vcc\n s_waitcnt lgkmcnt(0)\n s_sub_i32 %0, %1, vcc_lo\n"
#define RT_F RT_S "s_cmp_le_i32 %0, 4\n s_cbranch_scc1 2f\n"
  asm volatile("0: " RT_S "s_cmp_gt_i32 %0, 400\n s_cbranch_scc1 3f\n s_cmp_le_i32 %0, 13\n s_cbranch_scc1 1f\n s_sleep 1\n s_branch 0b\n"
               "1: " RT_F RT_F RT_F RT_F RT_F "s_branch 1b\n"
               "2: s_cmp_lt_i32 %0, 2\n s_cbranch_scc1 3f\n s_nop 2\n s_cmp_lt_i32 %0, 3\n s_cbranch_scc1 3f\n s_nop 2\n s_cmp_lt_i32 %0, 4\n s_cbranch_scc1 3f\n s_nop 6\n 3:"
               : "=&s"(r) : "s"(__builtin_amdgcn_readfirstlane(target)) : "vcc", "scc", "memory");
  return (unsigned)r - 1u < 4u ? target : target - (unsigned)r;
}
__device__ __forceinline__ unsigned ts_max(unsigned v) {
  if (__ballot(v == ~0u)) return ~0u;
  const unsigned base = (unsigned)__builtin_amdgcn_readfirstlane(v) - 4096u;
  unsigned d = v - base;
  d = max(d, (unsigned)__builtin_amdgcn_mov_dpp((int)d, 0x121, 0xf, 0xf, true)); d = max(d, (unsigned)__builtin_amdgcn_mov_dpp((int)d, 0x122, 0xf, 0xf, true));
  d = max(d, (unsigned)__builtin_amdgcn_mov_dpp((int)d, 0x124, 0xf, 0xf, true)); d = max(d, (unsigned)__builtin_amdgcn_mov_dpp((int)d, 0x128, 0xf, 0xf, true));
  return (base + max((unsigned)__builtin_amdgcn_readlane(d, 0), (unsigned)__builtin_amdgcn_readlane(d, 16)));
}
constexpr int TS_OFF = 32768 + 4096;
__device__ __forceinline__ unsigned ts_wait(__amdgpu_buffer_rsrc_t rs, int so) {
  unsigned v = ~0u;
  for (int n = 0; n < 64 && v == ~0u; ++n) { __builtin_amdgcn_s_sleep(2); asm volatile("" ::: "memory"); v = ts_max(__builtin_amdgcn_raw_buffer_load_b32(rs, (lane_id() & 31) * 128, so, 16)); }
  return v;
}
__device__ __forceinline__ int adapt(int d, unsigned failed, int base, int s, int up, int mask) {
  int r;
  asm volatile("s_add_i32 %0, %1, -1\n s_max_i32 %0, %0, %3\n s_and_b32 vcc_lo, %4, %6\n s_cselect_b32 %0, %1, %0\n s_add_i32 vcc_lo, %1, %5\n s_cmp_lg_u32 %2, 0\n s_cselect_b32 %0, vcc_lo, %0\n s_cmp_gt_i32 %4, 3\n s_cselect_b32 %0, %0, %1"
               : "=&s"(r) : "s"(__builtin_amdgcn_readfirstlane(d)), "s"(__builtin_amdgcn_readfirstlane(failed)), "s"(base), "s"(s), "s"(up), "s"(mask) : "vcc", "scc");
  return r;
}
#define ADAPT(d_, failed_, base_) d_ = adapt(d_, failed_, base_, s, a.d[5], a.d[6])
#define PINV(x) asm volatile("" : "+v"(x))
constexpr int L_MEM = 0, L_QI = 10240;
constexpr int L_B0 = 16384, L_B1 = 65536, L_B2 = 114688;
constexpr int L_KX = L_B1, L_FLG = 14080, L_ST = 14336, L_KI = L_B1 + 28672;

#define AFR(k_) (*(const u32x4*)(lds + afb + (k_) * 1024))

#define TR8_ASM "ds_read_b64_tr_b16 %0, %8\n ds_read_b64_tr_b16 %1, %8 offset:1536\n ds_read_b64_tr_b16 %2, %8 offset:12288\n ds_read_b64_tr_b16 %3, %8 offset:13824\n" \
               "ds_read_b64_tr_b16 %4, %8 offset:24576\n ds_read_b64_tr_b16 %5, %8 offset:26112\n ds_read_b64_tr_b16 %6, %8 offset:36864\n ds_read_b64_tr_b16 %7, %8 offset:38400\n s_waitcnt lgkmcnt(0)"
__device__ __forceinline__ void tr8(const unsigned ad, u32x8& f0, u32x8& f1) {
  u32x2 a, b, c, d, e, f, g, h;
  asm volatile(TR8_ASM : "=&v"(a), "=&v"(b), "=&v"(c), "=&v"(d), "=&v"(e), "=&v"(f), "=&v"(g), "=&v"(h) : "v"(ad) : "memory");
  f0 = (u32x8){a.x, a.y, b.x, b.y, c.x, c.y, d.x, d.y}; f1 = (u32x8){e.x, e.y, f.x, f.y, g.x, g.y, h.x, h.y};
}
__device__ __forceinline__ void glds(const void* sbase, unsigned voff, unsigned ldsdst) {
  unsigned keep;
  asm volatile("s_mov_b32 %0, m0\n s_mov_b32 m0, %3\n s_nop 0\n global_load_lds_dwordx4 %1, %2\n s_mov_b32 m0, %0" : "=&s"(keep) : "v"(voff), "s"(sbase), "s"(__builtin_amdgcn_readfirstlane(ldsdst)) : "memory");
}
__device__ __forceinline__ void touch(const void* p, unsigned ldsdump) {
  unsigned keep;
  asm volatile("s_mov_b32 %0, m0\n s_mov_b32 m0, %2\n s_nop 0\n global_load_lds_dword %1, off\n s_mov_b32 m0, %0" : "=&s"(keep) : "v"(p), "s"(__builtin_amdgcn_readfirstlane(ldsdump)) : "memory");
}
__device__ __forceinline__ unsigned lds_addr(const unsigned char* p) { return (unsigned)(size_t)(const LAS unsigned char*)p; }
__device__ __forceinline__ unsigned ld_word(const unsigned* p) { return (unsigned)__builtin_amdgcn_readfirstlane(__hip_atomic_load((const gu32*)p, RLX_AGENT)); }
template <int SLEEP>
__device__ __forceinline__ unsigned wait_ge(const unsigned* p, unsigned target, unsigned* tmo) {
  unsigned spins = 0, v;
  while ((v = ld_word(p)) < target) { __builtin_amdgcn_s_sleep(SLEEP); if (!spin_check(spins, tmo)) break; }
  asm volatile("" ::: "memory");
  return v;
}
constexpr int W_DONE = CNT(C_TOTAL), W_EPOCH = W_DONE + 128, W_SLOT = W_DONE + 256;
__device__ __forceinline__ void init_wait(const char* ws, unsigned tag) {
  const gu32* p = (const gu32*)(ws + O_CNT) + W_DONE + lane_id();
  for (unsigned n = 0; n < CH_SPIN_CAP && !__all(__hip_atomic_load(p, RLX_AGENT) == tag); ++n) __builtin_amdgcn_s_sleep(8);
  asm volatile("" ::: "memory");
}
template <int SLEEP>
__device__ __forceinline__ unsigned wait_t(const unsigned* p, const bool both, unsigned target, unsigned* tmo) {
  unsigned spins = 0, v;
  for (;;) {
    const u32x4 w = ld16(mkrs(p), 0u, 0u);
    v = min(min(w.x, w.y), w.z);
    if (both) { const u32x4 w2 = ld16(mkrs(p), 16u, 0u); v = min(v, min(min(w2.x, w2.y), w2.z)); }
    v = (unsigned)__builtin_amdgcn_readfirstlane(v);
    if (v >= target) break;
    __builtin_amdgcn_s_sleep(SLEEP); if (!spin_check(spins, tmo)) break;
  }
  asm volatile("" ::: "memory");
  return v;
}
__device__ __forceinline__ unsigned wait_u(const unsigned* p, unsigned target, unsigned* tmo) {
  unsigned spins = 0, v;
  for (;;) {
    const u32x4 w = ld16(mkrs(p), 0u, 0u);
    v = (unsigned)__builtin_amdgcn_readfirstlane(min(min(w.x, w.y), min(w.z, w.w)));
    if (v >= target) break;
    __builtin_amdgcn_s_sleep(4); if (!spin_check(spins, tmo)) break;
  }
  asm volatile("" ::: "memory");
  return v;
}
__device__ __forceinline__ unsigned min3u(unsigned a, unsigned b, unsigned c) { return min(min(a, b), c); }
template <int W>
__device__ __forceinline__ void s_role(const Args& a, unsigned char* lds, const int h, const unsigned tag) {
  char* ws = a.ws; asm volatile("" : "+s"(ws));
  unsigned* const cnt = (unsigned*)(ws + O_CNT); unsigned* const tmo = cnt + CNT(C_TMO);
  char* const X1 = ws + O_X1; char* const P = ws + O_PS;
  const int lane = lane_id(), l15 = lane & 15, lq = lane >> 4;
  constexpr int wave = W;
#define WG00 "{a[0:7]}"
#define WG01 "{a[8:15]}"
#define WG02 "{a[16:23]}"
#define WG03 "{a[24:31]}"
#define WG04 "{a[32:39]}"
#define WG05 "{a[40:47]}"
#define WG06 "{a[48:55]}"
#define WG07 "{a[56:63]}"
#define WG10 "{a[64:71]}"
#define WG11 "{a[72:79]}"
#define WG12 "{a[80:87]}"
#define WG13 "{a[88:95]}"
#define WG14 "{a[96:103]}"
#define WG15 "{a[104:111]}"
#define WG16 "{a[112:119]}"
#define WG17 "{a[120:127]}"
#define WG20 "{a[128:135]}"
#define WG21 "{a[136:143]}"
#define WG22 "{a[144:151]}"
#define WG23 "{a[152:159]}"
#define WG24 "{a[160:167]}"
#define WG25 "{a[168:175]}"
#define WG26 "{a[176:183]}"
#define WG27 "{a[184:191]}"
#define WG30 "{a[192:199]}"
#define WG31 "{a[200:207]}"
#define WG32 "{a[208:215]}"
#define WG33 "{a[216:223]}"
#define WG34 "{a[224:231]}"
#define WG35 "{a[232:239]}"
#define WG36 "{v[88:95]}"
#define WG37 "{v[96:103]}"
#define WG40 "{v[104:111]}"
#define WG41 "{v[112:119]}"
#define WG42 "{v[120:127]}"
#define WG43 "{v[128:135]}"
#define WG44 "{v[136:143]}"
#define WG45 "{v[144:151]}"
#define WG46 "{v[152:159]}"
#define WG47 "{v[160:167]}"
#define WG50 "{v[168:175]}"
#define WG51 "{v[176:183]}"
#define WG52 "{v[184:191]}"
#define WG53 "{v[192:199]}"
#define WG54 "{v[200:207]}"
#define WG55 "{v[208:215]}"
#define WG56 "{v[216:223]}"
#define WG57 "{v[224:231]}"
#define TRR(c, a, b, o) "ds_read_b64_tr_b16 " #c "[" #a ":" #b "], %2 offset:" #o "\n"
#define TW00 TRR(a, 0, 1, 0) TRR(a, 2, 3, 1536) TRR(a, 4, 5, 12288) TRR(a, 6, 7, 13824) TRR(a, 8, 9, 24576) TRR(a, 10, 11, 26112) TRR(a, 12, 13, 36864) TRR(a, 14, 15, 38400)
#define TW01 TRR(a, 16, 17, 0) TRR(a, 18, 19, 1536) TRR(a, 20, 21, 12288) TRR(a, 22, 23, 13824) TRR(a, 24, 25, 24576) TRR(a, 26, 27, 26112) TRR(a, 28, 29, 36864) TRR(a, 30, 31, 38400)
#define TW02 TRR(a, 32, 33, 0) TRR(a, 34, 35, 1536) TRR(a, 36, 37, 12288) TRR(a, 38, 39, 13824) TRR(a, 40, 41, 24576) TRR(a, 42, 43, 26112) TRR(a, 44, 45, 36864) TRR(a, 46, 47, 38400)
#define TW03 TRR(a, 48, 49, 0) TRR(a, 50, 51, 1536) TRR(a, 52, 53, 12288) TRR(a, 54, 55, 13824) TRR(a, 56, 57, 24576) TRR(a, 58, 59, 26112) TRR(a, 60, 61, 36864) TRR(a, 62, 63, 38400)
#define TW10 TRR(a, 64, 65, 0) TRR(a, 66, 67, 1536) TRR(a, 68, 69, 12288) TRR(a, 70, 71, 13824) TRR(a, 72, 73, 24576) TRR(a, 74, 75, 26112) TRR(a, 76, 77, 36864) TRR(a, 78, 79, 38400)
#define TW11 TRR(a, 80, 81, 0) TRR(a, 82, 83, 1536) TRR(a, 84, 85, 12288) TRR(a, 86, 87, 13824) TRR(a, 88, 89, 24576) TRR(a, 90, 91, 26112) TRR(a, 92, 93, 36864) TRR(a, 94, 95, 38400)
#define TW12 TRR(a, 96, 97, 0) TRR(a, 98, 99, 1536) TRR(a, 100, 101, 12288) TRR(a, 102, 103, 13824) TRR(a, 104, 105, 24576) TRR(a, 106, 107, 26112) TRR(a, 108, 109, 36864) TRR(a, 110, 111, 38400)
#define TW13 TRR(a, 112, 113, 0) TRR(a, 114, 115, 1536) TRR(a, 116, 117, 12288) TRR(a, 118, 119, 13824) TRR(a, 120, 121, 24576) TRR(a, 122, 123, 26112) TRR(a, 124, 125, 36864) TRR(a, 126, 127, 38400)
#define TW20 TRR(a, 128, 129, 0) TRR(a, 130, 131, 1536) TRR(a, 132, 133, 12288) TRR(a, 134, 135, 13824) TRR(a, 136, 137, 24576) TRR(a, 138, 139, 26112) TRR(a, 140, 141, 36864) TRR(a, 142, 143, 38400)
#define TW21 TRR(a, 144, 145, 0) TRR(a, 146, 147, 1536) TRR(a, 148, 149, 12288) TRR(a, 150, 151, 13824) TRR(a, 152, 153, 24576) TRR(a, 154, 155, 26112) TRR(a, 156, 157, 36864) TRR(a, 158, 159, 38400)
#define TW22 TRR(a, 160, 161, 0) TRR(a, 162, 163, 1536) TRR(a, 164, 165, 12288) TRR(a, 166, 167, 13824) TRR(a, 168, 169, 24576) TRR(a, 170, 171, 26112) TRR(a, 172, 173, 36864) TRR(a, 174, 175, 38400)
#define TW23 TRR(a, 176, 177, 0) TRR(a, 178, 179, 1536) TRR(a, 180, 181, 12288) TRR(a, 182, 183, 13824) TRR(a, 184, 185, 24576) TRR(a, 186, 187, 26112) TRR(a, 188, 189, 36864) TRR(a, 190, 191, 38400)
#define TW30 TRR(a, 192, 193, 0) TRR(a, 194, 195, 1536) TRR(a, 196, 197, 12288) TRR(a, 198, 199, 13824) TRR(a, 200, 201, 24576) TRR(a, 202, 203, 26112) TRR(a, 204, 205, 36864) TRR(a, 206, 207, 38400)
#define TW31 TRR(a, 208, 209, 0) TRR(a, 210, 211, 1536) TRR(a, 212, 213, 12288) TRR(a, 214, 215, 13824) TRR(a, 216, 217, 24576) TRR(a, 218, 219, 26112) TRR(a, 220, 221, 36864) TRR(a, 222, 223, 38400)
#define TW32 TRR(a, 224, 225, 0) TRR(a, 226, 227, 1536) TRR(a, 228, 229, 12288) TRR(a, 230, 231, 13824) TRR(a, 232, 233, 24576) TRR(a, 234, 235, 26112) TRR(a, 236, 237, 36864) TRR(a, 238, 239, 38400)
#define TW33 TRR(v, 88, 89, 0) TRR(v, 90, 91, 1536) TRR(v, 92, 93, 12288) TRR(v, 94, 95, 13824) TRR(v, 96, 97, 24576) TRR(v, 98, 99, 26112) TRR(v, 100, 101, 36864) TRR(v, 102, 103, 38400)
#define TW40 TRR(v, 104, 105, 0) TRR(v, 106, 107, 1536) TRR(v, 108, 109, 12288) TRR(v, 110, 111, 13824) TRR(v, 112, 113, 24576) TRR(v, 114, 115, 26112) TRR(v, 116, 117, 36864) TRR(v, 118, 119, 38400)
#define TW41 TRR(v, 120, 121, 0) TRR(v, 122, 123, 1536) TRR(v, 124, 125, 12288) TRR(v, 126, 127, 13824) TRR(v, 128, 129, 24576) TRR(v, 130, 131, 26112) TRR(v, 132, 133, 36864) TRR(v, 134, 135, 38400)
#define TW42 TRR(v, 136, 137, 0) TRR(v, 138, 139, 1536) TRR(v, 140, 141, 12288) TRR(v, 142, 143, 13824) TRR(v, 144, 145, 24576) TRR(v, 146, 147, 26112) TRR(v, 148, 149, 36864) TRR(v, 150, 151, 38400)
#define TW43 TRR(v, 152, 153, 0) TRR(v, 154, 155, 1536) TRR(v, 156, 157, 12288) TRR(v, 158, 159, 13824) TRR(v, 160, 161, 24576) TRR(v, 162, 163, 26112) TRR(v, 164, 165, 36864) TRR(v, 166, 167, 38400)
#define TW50 TRR(v, 168, 169, 0) TRR(v, 170, 171, 1536) TRR(v, 172, 173, 12288) TRR(v, 174, 175, 13824) TRR(v, 176, 177, 24576) TRR(v, 178, 179, 26112) TRR(v, 180, 181, 36864) TRR(v, 182, 183, 38400)
#define TW51 TRR(v, 184, 185, 0) TRR(v, 186, 187, 1536) TRR(v, 188, 189, 12288) TRR(v, 190, 191, 13824) TRR(v, 192, 193, 24576) TRR(v, 194, 195, 26112) TRR(v, 196, 197, 36864) TRR(v, 198, 199, 38400)
#define TW52 TRR(v, 200, 201, 0) TRR(v, 202, 203, 1536) TRR(v, 204, 205, 12288) TRR(v, 206, 207, 13824) TRR(v, 208, 209, 24576) TRR(v, 210, 211, 26112) TRR(v, 212, 213, 36864) TRR(v, 214, 215, 38400)
#define TW53 TRR(v, 216, 217, 0) TRR(v, 218, 219, 1536) TRR(v, 220, 221, 12288) TRR(v, 222, 223, 13824) TRR(v, 224, 225, 24576) TRR(v, 226, 227, 26112) TRR(v, 228, 229, 36864) TRR(v, 230, 231, 38400)
#define TWL(n, j, k0, k1, ad_) asm volatile(TW##n##j "s_waitcnt lgkmcnt(0)" : "=&" WG##n##k0(wt[n][k0]), "=&" WG##n##k1(wt[n][k1]) : "v"(ad_) : "memory")
#define TWN(n, j_, ad_) do { if ((j_) == 0) TWL(n, 0, 0, 1, ad_); else if ((j_) == 1) TWL(n, 1, 2, 3, ad_); else if ((j_) == 2) TWL(n, 2, 4, 5, ad_); else TWL(n, 3, 6, 7, ad_); } while (0)
  u32x8 wt[6][8];
  unsigned tro[3];
  {
    const unsigned row = (unsigned)(8 * lq + (l15 >> 2)), swz = ((row >> 1) & 1u) * 2u + ((row >> 3) & 1u) * 4u;
#pragma unroll
    for (int t = 0; t < 3; ++t) tro[t] = row * 384u + (((unsigned)(2 * (4 * t + wave) + ((l15 & 3) >> 1))) ^ swz) * 16u + (unsigned)(l15 & 1) * 8u;
  }
#define SBUF(i_) (((i_) % 3) == 0 ? L_B0 : (((i_) % 3) == 1 ? L_B1 : L_B2))
  unsigned svo[12]; const int sq0 = 3 * ((h >> 2) & 3);
#pragma unroll
  for (int q_ = 0; q_ < 12; ++q_) { const int qr_ = q_ + sq0, ins = wave + 4 * (qr_ < 12 ? qr_ : qr_ - 12); const unsigned c = (unsigned)(ins * 64 + lane), r = c / 24u, pc = c - r * 24u, sw = ((r >> 1) & 1u) * 2u + ((r >> 3) & 1u) * 4u;
    svo[q_] = ((r & ~10u) | ((r & 2u) << 2) | ((r & 8u) >> 2)) * 12288u + 16u * (pc ^ sw); PINV(svo[q_]); }
#define SDMA(i_) do { const char* W_ = (const char*)((((i_) & 1) ? a.Wk : a.Wq) + (size_t)(128 * (((i_) >> 1) < 4 ? ((((i_) >> 1) + h) & 3) : 4)) * 6144 + 192 * h);        \
    _Pragma("unroll") for (int q_ = 0; q_ < 12; ++q_) { const int qr_ = q_ + sq0; glds(W_, svo[q_], lds_addr(lds) + (unsigned)(SBUF(i_) + (wave + 4 * (qr_ < 12 ? qr_ : qr_ - 12)) * 1024)); } SB(); } while (0)
  SDMA(0); SDMA(1); SDMA(2);
#pragma unroll
  for (int i = 0; i < 10; ++i) {
    if (i < 8) VMW(24); else if (i == 8) VMW(12); else { init_wait(ws, tag); VMW(0); }
    BAR();
    u32x8 res[3][2];
    const unsigned tb = lds_addr(lds) + (unsigned)SBUF(i); const int sl = ((i >> 1) - W) & 3;
    if (i >= 8) {
#pragma unroll
      for (int t = 0; t < 3; ++t) tr8(tb + tro[t], res[t][0], res[t][1]);
    } else if (i & 1) { TWN(3, sl, tb + tro[0]); TWN(4, sl, tb + tro[1]); TWN(5, sl, tb + tro[2]); }
    else { TWN(0, sl, tb + tro[0]); TWN(1, sl, tb + tro[1]); TWN(2, sl, tb + tro[2]); }
    BAR();
    if (i + 3 < 10) SDMA(i + 3);
    if (i >= 8) {
#pragma unroll
      for (int t = 0; t < 3; ++t)
#pragma unroll
        for (int k = 0; k < 2; ++k) {
          unsigned char* dst = lds + SBUF(i) + wave * 12288 + t * 4096 + k * 2048 + lane * 16;
          *(u32x4*)dst = lo4(res[t][k]); *(u32x4*)(dst + 1024) = hi4(res[t][k]);
        }
    }
  }
  const unsigned pol = wait_ge<8>(cnt + CNT(C_POL), 1u, tmo) & 1u;
  const __amdgpu_buffer_rsrc_t rs_p = mkrs(P), rs_kf = mkrs(ws + O_KF + (size_t)h * QK_H), rs_tp = mkrs(cnt + CNT(C_PT_ + h));
  unsigned tprog = 0;
  constexpr int kxs = 6 * W;
  if (W < 2) {
    tprog = wait_t<4>(cnt + CNT(C_PT_ + h), false, 1u, tmo);
#pragma unroll
    for (int i = 0; i < 6; ++i) *(u32x4*)(lds + L_KX + (kxs + i) * 1024 + lane * 16) = ld16(rs_kf, (unsigned)((kxs + i) * 1024 + lane * 16), 0u);
  } else if (W == 2) { if (lane < 32) *(unsigned*)(lds + L_FLG + 4 * lane) = lane == 24 ? DUR0 + 8u : 0u; *(u32x4*)(lds + L_ST + lane * 16) = (u32x4){0u, 0u, 0u, 0u}; }
  const unsigned lb = lds_addr(lds);
  unsigned afb = lb + (unsigned)(lane * 16), trq = (unsigned)(L_B2 + wave * 12288) + afb; PINV(trq);
#define trk (afb + (unsigned)(L_B0 + wave * 12288))
#define LP(T_, ad_) ((LAS T_*)(ad_))
  const __amdgpu_buffer_rsrc_t rs_x1 = mkrs(X1 - lb), rs_kn = mkrs(ws + O_KF + (size_t)h * QK_H + kxs * 1024 - lb);
  int ix = l15 < 8 ? 0x4444 : 0xEEEE; PINV(ix); int ix2 = (l15 & 2) ? 0xEEEE : 0x4444;
  const unsigned cp = (unsigned)(2 * (lq & 1) + (lq >> 1));
  unsigned qwa = lb + (unsigned)(L_QI + wave * 320 + ((l15 >> 2) * 2 + (l15 & 1)) * 32 + ((l15 >> 1) & 1) * 4) + cp * 8u, kwa = lb + (unsigned)(L_KI + (16 * wave + l15) * 16) + cp * 4u;
  unsigned qra = lb + (unsigned)(L_QI + (2 * (lq >> 1) + (l15 >> 3)) * 320 + (lq & 1) * 128 + ((l15 >> 2) & 1) * 32 + (l15 & 3) * 8);
  unsigned kra = lb + (unsigned)(L_KI + (4 * lq + (l15 >> 2)) * 16 + (l15 & 1) * 8);
  unsigned lwa = lb + (unsigned)(L_ST + 2 * lq * 128 + (16 * wave + l15) * 4), fl0 = lb + (unsigned)L_FLG;
  PINV(afb); PINV(qwa); PINV(kwa); PINV(fl0); if (W < 3) { PINV(qra); PINV(ix2); } if (W >= 2) PINV(kra); if (W < 2) PINV(lwa);
  unsigned kxb = (unsigned)(L_KX + wave * 6144) + afb; if (W < 2) PINV(kxb);
  const bool hi8 = l15 >= 8;
  const __amdgpu_buffer_rsrc_t rs_ts = mkrs(P + half_of(P, 0u, 0u, pol) + TS_OFF);
  const __amdgpu_buffer_rsrc_t rs_ps = mkrs(cnt + CNT(C_PS));
  int tso = lane == 0 ? 128 * h : (int)0x80000000u; if (W == 3) PINV(tso);
  unsigned po = side_map(P, (unsigned)((4 * h + ((lane & 7) >> 1)) * 128 + (lane >> 3) * 16 + (lane & 1) * 8), 0u, pol);
  unsigned pm = (lane & 1) == 0 ? side_map(P, (unsigned)(16384 + h * 128 + lane * 2), 0u, pol) : 0x80000000u;
  float lsc = 1.4426950408889634f * 0.07216878364870323f;
  if (W == 2) { PINV(po); PINV(pm); asm volatile("" : "+s"(lsc)); }
  const unsigned xh = half_of(X1, 0u, 0u, pol), gb = (unsigned)((W + h) & 3) * 2048u;
  unsigned lg0 = 0, tb2 = 0, dur = DUR0, tsv = ~0u; int dly = a.d[0] + 2 * a.d[4]; asm volatile("" : "+s"(dly));
  BAR();
  constexpr int P0 = 2 * W, P1 = 2 * W + 1, R2 = W >> 1;
#define OT(i_) ((2 * W + 2 + (i_)) & 7)
#define AR0 "v[232:235]"
#define AR1 "v[236:239]"
#define AR2 "v[240:243]"
#define AR3 "v[244:247]"
#define AR4 "v[248:251]"
#define AR5 "v[252:255]"
#define SW01 "v_permlane32_swap_b32 v232, v234\n"
#define SW02 "v_permlane32_swap_b32 v233, v235\n"
#define SW11 "v_permlane32_swap_b32 v236, v238\n"
#define SW12 "v_permlane32_swap_b32 v237, v239\n"
#define SW21 "v_permlane32_swap_b32 v240, v242\n"
#define SW22 "v_permlane32_swap_b32 v241, v243\n"
#define SW31 "v_permlane32_swap_b32 v244, v246\n"
#define SW32 "v_permlane32_swap_b32 v245, v247\n"
#define SW41 "v_permlane32_swap_b32 v248, v250\n"
#define SW42 "v_permlane32_swap_b32 v249, v251\n"
#define SMI "v_smfmac_f32_16x16x64_bf16 %0, %1, %2, %3"
#define SMC(M_, c_, ...) do { if ((c_) == 0) M_("v", __VA_ARGS__); else if ((c_) == 2) M_("{a[240:247]}", __VA_ARGS__); else M_("{a[248:255]}", __VA_ARGS__); SB(); } while (0)
#define SM_(bc, n, f_, w_, x_) asm volatile(SMI : "+{" AR##n "}"(ac[n]) : "v"(f_), bc(w_), "v"(x_))
#define SMP_(bc, n, f_, w_, x_, m, p) asm volatile(SW##m##p "v_smfmac_f32_16x16x64_bf16 %0, %2, %3, %4" : "+{" AR##n "}"(ac[n]), "+{" AR##m "}"(ac[m]) : "v"(f_), bc(w_), "v"(x_))
#define RC(j_) ((j_) == 1 ? 2 : ((j_) == 3 ? 3 : 0))
#define MW(n, f_, k) do { SM_(WG##n##k, n, f_, wt[n][k], ix); SB(); } while (0)
#define MO(n, k) MW(n, A[k - 2], k)
#define MR(n, f_, j_) SMC(SM_, RC(j_), n, f_, r##j_, ix)
#define MOP(n, k, m, p) do { SMP_(WG##n##k, n, A[k - 2], wt[n][k], ix, m, p); SB(); } while (0)
#define MRP(n, f_, j_, m, p) SMC(SMP_, RC(j_), n, f_, r##j_, ix, m, p)
#define SL(i_, j_) SMC(SM_, RC(j_), 0, qf[i_], r##j_, ix2)
#define SLP(i_, j_, m, p) SMC(SMP_, RC(j_), 0, qf[i_], r##j_, ix2, m, p)
#define APIN(n) asm volatile("" : "+{" AR##n "}"(ac[n]))
#define ZL() do { asm volatile("v_mov_b64 v[232:233], 0\n v_mov_b64 v[234:235], 0" : "={v[232:235]}"(ac[0])); SB(); } while (0)
#define RLO(tb_, t_, k2_) (*LP(const u32x4, (tb_) + (t_) * 4096 + (k2_) * 2048))
#define RHI(tb_, t_, k2_) (*LP(const u32x4, (tb_) + (t_) * 4096 + (k2_) * 2048 + 1024))
#define SFR(k_) (*LP(const u32x4, afb + (k_) * 1024))
#define TR1(ad_) __builtin_bit_cast(u32x2, __builtin_amdgcn_ds_read_tr16_b64_v4i16(LP(bf16x4, ad_)))
#define TR2(ad_, d_) __builtin_shufflevector(TR1(ad_), TR1((ad_) + (d_)), 0, 1, 2, 3)
#define RFR(tb_, t_, k2_) cat8(RLO(tb_, t_, k2_), RHI(tb_, t_, k2_))
#define DS(x) do { x; SB(); } while (0)
#define VV(v_, x) do { v_ = (x); PINV(v_); SB(); } while (0)
#define FLG(st_) do { asm volatile("" ::: "memory"); *LP(volatile unsigned, fl0 + (st_) * 16 + W * 4) = snv; SB(); } while (0)
#define FLGV(st_, v_) do { asm volatile("" ::: "memory"); *LP(volatile unsigned, fl0 + (st_) * 16 + W * 4) = v_; SB(); } while (0)
#define MEET() do { asm volatile("s_barrier" ::: "memory"); SB(); } while (0)
#define FRD(g_, st_) do { asm volatile("" ::: "memory"); g_ = *LP(const volatile u32x4, fl0 + (st_) * 16); asm volatile("" ::: "memory"); SB(); } while (0)
#define FCK(g_) do { mn = min3u(mn, g_.x, g_.y); mn = min3u(mn, g_.z, g_.w); PINV(mn); SB(); } while (0)
#define AD0(n_) VV(e0, ac[n_][0] + ac[n_][2])
#define AD1(n_) VV(e1, ac[n_][1] + ac[n_][3])
#define CVT(w_) VV(w_, cvtpk(e0, e1))
#define FOLD5() do { asm volatile("s_nop 7\n v_permlane32_swap_b32 v252, v254\n v_permlane32_swap_b32 v253, v255\n v_add_f32 v252, v252, v254\n v_add_f32 v253, v253, v255\n v_cvt_pk_bf16_f32 v252, v252, v253" : "+{" AR5 "}"(ac[5])); SB(); } while (0)
#define WR(ad_, w_) DS(*LP(unsigned, ad_) = w_)
#define QFR(j_) qf[j_] = TR2(qra + (j_) * 1280, 64u)
#define KMR(j_) cat8(TR2(kra + (j_) * 1024, 256u), TR2(kra + (j_) * 1024 + 512, 256u))
#define KXR(j_) RFR(kxb, 0, j_)
#pragma unroll 1
  for (int s = 0; s < 64; ++s) {
    u32x8 r0 = RFR(trq, 0, R2), r1 = RFR(trq, 1, R2), r2 = RFR(trq, 2, R2), r3 = RFR(trq, 0, 1 - R2);
    int pss = PSLOT(s); asm volatile("" : "+s"(pss));
    f32x4 ac[6] = {ZERO4, ZERO4, ZERO4, ZERO4, ZERO4, ZERO4};
    unsigned snv = (unsigned)(s + 1);
    unsigned so = xh + (unsigned)s * X1_STEP, so2 = so + 16384u + (unsigned)R2 * 1024u; so += gb; unsigned so1 = so + 1024u;
    asm volatile("" : "+v"(snv), "+s"(so), "+s"(so1), "+s"(so2));
    APIN(0); APIN(1); APIN(2); APIN(3); APIN(4); APIN(5);
    lg0 = wait_rt(tsv + (unsigned)dly);
    u32x4 g0, g1, g2; unsigned m, spins = 0;
#define G_OWN(H_) do { g0 = ld16(rs_x1, afb, so); SB(); g1 = ld16(rs_x1, afb, so1); SB(); g2 = ld16(rs_x1, afb, so2); SB(); H_; SB(); \
      MW(0, g0, 0); DS(*LP(u32x4, afb + P0 * 1024) = g0); \
      MW(1, g0, 0); VV(m, max(g0.x, g0.z)); \
      MW(2, g0, 0); DS(*LP(u32x4, afb + P1 * 1024) = g1); \
      MW(0, g1, 1); VV(m, max(max(m, g1.x), g1.z)); \
      MW(1, g1, 1); if ((W & 1) == 0) DS(*LP(u32x4, afb + (8 + R2) * 1024) = g2); VV(m, max(max(m, g2.x), g2.z)); \
      MW(2, g1, 1); } while (0)
    G_OWN(if (W == 3) { __builtin_amdgcn_raw_buffer_store_b32(min(lg0 + dur, 0xfffffffeu), rs_ts, tso, pss, 16); VMW(4);        \
      __builtin_amdgcn_raw_buffer_store_b32((unsigned)s, rs_ps, tso, 0, 16); });
    if (__builtin_expect(__ballot(m == ~0u) != 0ull, 0)) {

      const int to = lane_id() == 0 ? 128 * h : (int)0x80000000u; unsigned tn;
      for (;;) {
        __builtin_amdgcn_raw_buffer_store_b32(~0u, rs_ts, to, pss, 16);
        if (spins > 2u) __builtin_amdgcn_s_sleep(BKO);
        ac[0] = ZERO4; ac[1] = ZERO4; ac[2] = ZERO4; APIN(0); APIN(1); APIN(2);
        tn = (unsigned)__builtin_amdgcn_s_memrealtime() + *LP(const volatile unsigned, fl0 + 96);
        G_OWN((void)0);
        const bool go = spin_check(spins, tmo); if (__ballot(m == ~0u) == 0ull || !go) break; }
      __builtin_amdgcn_raw_buffer_store_b32(min(tn, 0xfffffffeu), rs_ts, to, pss, 16);
    }
    MR(0, g2, 0);
    BAR();
    u32x4 A[7], lo, hi; float e0, e1; unsigned w0, w1, w2; u32x8 r4;
    DS(A[0] = SFR(OT(0)));
    MR(1, g2, 1); DS(A[1] = SFR(OT(1)));
    MR(2, g2, 2); DS(A[2] = SFR(OT(2)));
    MW(3, g0, 0); DS(A[3] = SFR(OT(3)));
    MW(4, g0, 0); DS(A[4] = SFR(OT(4)));
    MW(5, g0, 0); DS(A[5] = SFR(OT(5)));
    MW(3, g1, 1); DS(A[6] = SFR(9 - R2));
    MW(4, g1, 1); DS(lo = RLO(trq, 1, 1 - R2));
    MW(5, g1, 1); DS(hi = RHI(trq, 1, 1 - R2)); r0 = cat8(lo, hi);
    MO(0, 2); DS(lo = RLO(trq, 2, 1 - R2));
    MO(0, 3); DS(hi = RHI(trq, 2, 1 - R2)); r1 = cat8(lo, hi);
    MO(0, 4); DS(lo = RLO(trk, 0, R2));
    MO(0, 5); DS(hi = RHI(trk, 0, R2)); r2 = cat8(lo, hi);
    MO(0, 6); DS(lo = RLO(trk, 0, 1 - R2));
    MO(0, 7); DS(hi = RHI(trk, 0, 1 - R2)); r4 = cat8(lo, hi);
    MR(0, A[6], 3);
    MR(1, A[6], 0); DS(lo = RLO(trk, 1, R2));
    MO(1, 2);
    MOP(1, 3, 0, 1);
    MOP(1, 4, 0, 2); AD0(0);
    MO(1, 5); AD1(0);
    MO(1, 6); CVT(w0);
    MO(1, 7); DS(hi = RHI(trk, 1, R2)); r3 = cat8(lo, hi);
    MR(2, A[6], 1); DS(lo = RLO(trk, 1, 1 - R2));
    MO(2, 2);
    MOP(2, 3, 1, 1);
    MOP(2, 4, 1, 2); AD0(1);
    MO(2, 5); AD1(1);
    MO(2, 6); CVT(w1);
    MO(2, 7); DS(hi = RHI(trk, 1, 1 - R2)); r0 = cat8(lo, hi);
    MR(3, g2, 2); if (W >= 2) DS(lo = RLO(trk, 2, R2));
    MR(3, A[6], 4);
    MRP(4, g2, 3, 2, 1);
    MOP(3, 2, 2, 2); AD0(2); AD1(2);
    MO(3, 3); WR(qwa, w0); WR(qwa + 1280, w1); CVT(w2); WR(qwa + 2560, w2); FLG(0);
    u32x4 qf[3], g; unsigned mn = snv;
    if (W < 2) {
      u32x4 l2;
      MO(3, 4); DS(l2 = RLO(kxb, 0, 0));
      MO(3, 5); DS(hi = RHI(kxb, 0, 0)); r4 = cat8(l2, hi);
      MO(3, 6); DS(l2 = RLO(kxb, 0, 1));
      MO(3, 7); DS(hi = RHI(kxb, 0, 1)); r3 = cat8(l2, hi);
      MR(4, A[6], 0); FRD(g, 0); DS(QFR(0));
      MO(4, 2); DS(QFR(1));
      MOP(4, 3, 3, 1); DS(QFR(2));
      MOP(4, 4, 3, 2); DS(l2 = RLO(kxb, 0, 2)); AD0(3);
      MO(4, 5); DS(hi = RHI(kxb, 0, 2)); r0 = cat8(l2, hi); AD1(3);
      MO(4, 6); VV(mn, min3u(mn, g.x, g.y)); ZL();
      MO(4, 7); VV(mn, min3u(mn, g.z, g.w));
      SL(0, 4); CVT(w0);
      SL(1, 3); DS(lo = RLO(trk, 2, R2));
      SLP(2, 0, 4, 1); DS(hi = RHI(trk, 2, R2)); r1 = cat8(lo, hi);
      MOP(5, 2, 4, 2); DS(lo = RLO(trk, 2, 1 - R2)); AD0(4);
      MO(5, 3); DS(hi = RHI(trk, 2, 1 - R2)); r2 = cat8(lo, hi); AD1(4);
      MO(5, 4); CVT(w1); AD0(0); AD1(0);
      MO(5, 5); DS(*LP(float, lwa) = e0; *LP(float, lwa + 128) = e1); FLGV(4, mn); WR(kwa, w0); WR(kwa + 1024, w1); FLG(2);
      MO(5, 6); MO(5, 7); MEET();
      MR(5, g2, 1); MR(5, A[6], 2);
      FOLD5(); WR(kwa + 2048, __float_as_uint(ac[5][0])); FLG(3); MEET();
      if (__builtin_expect(__ballot(mn != snv) != 0ull, 0)) {
        int n_ = 0; do { mn = snv; FRD(g, 0); FCK(g); } while (__ballot(mn != snv) != 0ull && ++n_ < 65536);
        QFR(0); QFR(1); QFR(2); r4 = KXR(0); r3 = KXR(1); r0 = KXR(2); ZL(); asm volatile("s_nop 1"); SL(0, 4); SL(1, 3); SL(2, 0);
        asm volatile("s_nop 7" : "+{" AR0 "}"(ac[0]));
        AD0(0); AD1(0);
        *LP(float, lwa) = e0; *LP(float, lwa + 128) = e1; FLG(4);
      }
    } else {
      MO(3, 4); DS(hi = RHI(trk, 2, R2)); r1 = cat8(lo, hi);
      MO(3, 5); DS(lo = RLO(trk, 2, 1 - R2));
      MO(3, 6); DS(hi = RHI(trk, 2, 1 - R2)); r2 = cat8(lo, hi);
      MO(3, 7);
      MR(4, A[6], 0); if (W == 2) FRD(g, 0);
      MO(4, 2);
      MOP(4, 3, 3, 1);
      MOP(4, 4, 3, 2); if (W == 2) DS(QFR(0)); AD0(3);
      MO(4, 5); if (W == 2) DS(QFR(1)); AD1(3);
      MO(4, 6); if (W == 2) DS(QFR(2)); CVT(w0);
      MO(4, 7); if (W == 2) VV(mn, min3u(mn, g.x, g.y));
      MR(5, g2, 1); if (W == 2) VV(mn, min3u(mn, g.z, g.w));
      MR(5, A[6], 2);
      MOP(5, 2, 4, 1);
      MOP(5, 3, 4, 2); AD0(4);
      MO(5, 4); AD1(4);
      MO(5, 5); WR(kwa, w0); CVT(w1); WR(kwa + 1024, w1); FLG(2);
      MO(5, 6); if (W == 2) ZL();
      MO(5, 7);
      FOLD5(); WR(kwa + 2048, __float_as_uint(ac[5][0])); FLG(3);
      MEET();
      if (W == 3) MEET();
      if (W == 2) {
      u32x4 g3; u32x2 g4; f32x4 xv;
#define XRD() do { asm volatile("" ::: "memory"); g4 = *LP(const volatile u32x2, fl0 + 64); asm volatile("" ::: "memory"); xv = *LP(const volatile f32x4, afb + L_ST); SB(); } while (0)
      XRD();
      FRD(g, 2); DS(r0 = KMR(0)); DS(r1 = KMR(1));
      MEET();
      FRD(g3, 3); DS(r2 = KMR(2));
      float ml, x0, x1, x2, x3;
      constexpr float LS = 1.4426950408889634f * 0.07216878364870323f;
#define TOKM() VV(ml, fmx(fmaxf(fmaxf(xv[0], xv[1]), xv[2]), xv[3]))
      TOKM();
      VV(x0, xv[0] - ml);
      SL(0, 0); VV(x1, xv[1] - ml);
      SL(1, 1); VV(x2, xv[2] - ml); VV(x3, xv[3] - ml); VV(x0, x0 * LS); VV(x1, x1 * LS);
      VV(mn, min3u(mn, g4.x, g4.y)); FCK(g);
      SL(2, 2);
      FCK(g3);
      VV(x0, __builtin_amdgcn_exp2f(x0)); VV(x1, __builtin_amdgcn_exp2f(x1));
      if (__builtin_expect(__ballot(mn != snv) != 0ull, 0)) {
        int n_ = 0;
        do { FRD(g3, 3); XRD(); mn = min3u(snv, g4.x, g4.y); FCK(g3); } while (__ballot(mn != snv) != 0ull && ++n_ < 65536);
        QFR(0); QFR(1); QFR(2);
        ZL(); r0 = KMR(0); r1 = KMR(1); r2 = KMR(2); SB(); SL(0, 0); SL(1, 1); SL(2, 2);
        TOKM();
        x0 = __builtin_amdgcn_exp2f(nopk(xv[0] - ml) * LS); x1 = __builtin_amdgcn_exp2f(nopk(xv[1] - ml) * LS); x2 = xv[2] - ml; x3 = xv[3] - ml;
        asm volatile("s_nop 7" : "+v"(x0), "+v"(x1), "+v"(x2), "+v"(x3));
      }
      u32x2 pt; unsigned pmw;
#define DP(c_) " " c_ " row_mask:0xf bank_mask:0xf\n"
      asm volatile("v_mul_f32 %5, %8, %5\n v_mul_f32 %6, %8, %6\n"
                   "v_add_f32_dpp v240, v232, v234 quad_perm:[0,1,2,3] row_mask:0xf bank_mask:0x3\n v_add_f32_dpp v240, v233, v235 quad_perm:[0,1,2,3] row_mask:0xf bank_mask:0xc\n"
                   "v_max_f32 v241, v240, %7\n v_exp_f32 %5, %5\n v_exp_f32 %6, %6\n"
                   "v_max_f32_dpp v241, v241, v241" DP("quad_perm:[1,0,3,2]") "v_add_f32 v242, %3, %4\n v_add_f32 v243, %5, %6\n"
                   "v_max_f32_dpp v241, v241, v241" DP("quad_perm:[2,3,0,1]") "v_add_f32 v242, v242, v243\n s_nop 0\n"
                   "v_max_f32_dpp v241, v241, v241" DP("row_half_mirror")
                   "v_sub_f32 v240, v240, v241\n v_sub_f32 v241, %7, v241\n v_mul_f32 v240, %8, v240\n v_mul_f32 v241, %8, v241\n v_exp_f32 v240, v240\n v_exp_f32 v241, v241\n s_nop 0\n"
                   "v_fma_f32 v242, v242, v241, v240\n v_mul_f32 %3, %3, v241\n v_mul_f32 %4, %4, v241\n"
                   "v_add_f32_dpp v242, v242, v242" DP("quad_perm:[1,0,3,2]") "v_mul_f32 %5, %5, v241\n v_mul_f32 %6, %6, v241\n"
                   "v_add_f32_dpp v242, v242, v242" DP("quad_perm:[2,3,0,1]") "v_mov_b32_dpp v243, v240" DP("quad_perm:[1,0,3,2]") "s_nop 0\n"
                   "v_add_f32_dpp v242, v242, v242" DP("row_half_mirror")
                   "v_rcp_f32 v242, v242\n s_nop 0\n"
                   "v_mul_f32 %3, %3, v242\n v_mul_f32 %4, %4, v242\n v_mul_f32 %5, %5, v242\n v_mul_f32 %6, %6, v242\n"
                   "v_cvt_pk_bf16_f32 v236, |%3|, |%4|\n v_cvt_pk_bf16_f32 v237, |%5|, |%6|\n"
                   "buffer_store_dwordx2 v[236:237], %9, %11, %12 offen sc1\n"
                   "v_mul_f32 v240, v240, v242\n v_mul_f32 v243, v243, v242\n v_cvt_pk_bf16_f32 %1, |v240|, |v243|\n"
                   "buffer_store_dword %1, %10, %11, %12 offen sc1"
                   : "=&{v[236:237]}"(pt), "=&v"(pmw), "+{" AR0 "}"(ac[0]), "+v"(x0), "+v"(x1), "+v"(x2), "+v"(x3)
                   : "v"(ml), "s"(lsc), "v"(po), "v"(pm), "s"(rs_p), "s"(pss) : "v240", "v241", "v242", "v243", "memory");
      SB();
      u64 t2; RT_MARK(t2);
      *LP(volatile u32x2, fl0 + 80) = (u32x2){(unsigned)t2, snv}; SB();
      }
    }
#define LWAIT(v_, off_) do { int n_ = 0; for (;;) { v_ = *LP(const volatile u32x2, fl0 + (off_)); if ((unsigned)__builtin_amdgcn_readfirstlane(v_.y) == (unsigned)(s + 1) || ++n_ >= (1 << 20)) break; __builtin_amdgcn_s_sleep(1); } asm volatile("" ::: "memory"); } while (0)
    if (W < 2) {
      u32x4 v[6], tpl; unsigned tsl;
      const int sn = s < 63 ? s + 1 : 63;
      if (W == 0) { tsl = __builtin_amdgcn_raw_buffer_load_b32(rs_ts, (lane_id() & 31) * 128, pss, 16); SB(); }
      if (__builtin_expect(tprog < (unsigned)(sn + 1), 0)) tprog = wait_t<4>(cnt + CNT(C_PT_ + h), false, (unsigned)(sn + 1), tmo);
#pragma unroll
      for (int i = 0; i < 6; ++i) v[i] = ld16(rs_kn, afb, (unsigned)sn * QK_S + (unsigned)(i * 1024));
      tpl = ld16(rs_tp, 0u, 0u);
      SB();
      if (W == 0) {
        tsv = ts_max(tsl);
        if (__builtin_expect(tsv == ~0u, 0) && s < 63) tsv = ts_wait(rs_ts, pss);
        *LP(volatile u32x2, fl0 + 104) = (u32x2){tsv, snv}; SB();
      }
#pragma unroll
      for (int i = 0; i < 6; ++i) *LP(u32x4, kxb + i * 1024) = v[i];
      tprog = (unsigned)__builtin_amdgcn_readfirstlane(min3u(tpl.x, tpl.y, tpl.z));
    }
    if (W == 3) {
      const int ln = lane_id();
      unsigned ko = (unsigned)(((ln >> 3) & 1) * 512 + (ln >> 4) * 128 + (ln & 7) * 16);
      unsigned eo = ln < 32 ? side_map(P, (unsigned)(h * 512 + ln * 16), 0u, pol) : (ln < 48 ? side_map(P, (unsigned)(16384 + (ln & 8) * 512 + h * 128 + (ln & 7) * 16), 0u, pol) : 0x80000000u);
      PINV(ko); PINV(eo); SB();
      u32x4 kv[3];
      { int n_ = 0; do { mn = snv; FRD(g, 3); FCK(g); } while (__builtin_expect(__ballot(mn != snv) != 0ull, 0) && ++n_ < 65536); }
#pragma unroll
      for (int kk = 0; kk < 3; ++kk) {
        const u32x4 v1 = TR2(kra + kk * 1024, 256u), v2 = TR2(kra + kk * 1024 + 512, 256u);
        kv[kk] = (u32x4){min(hi8 ? v2.x : v1.x, 0xfffffffeu), hi8 ? v2.y : v1.y, min(hi8 ? v2.z : v1.z, 0xfffffffeu), hi8 ? v2.w : v1.w};
      }
      asm volatile("" : "+v"(kv[0]), "+v"(kv[1]), "+v"(kv[2])); SB();
      u32x2 tt = {0u, 0u};
      if (s < 63) LWAIT(tt, 80);
      if (XSL) __builtin_amdgcn_s_sleep(XSL);
      const __amdgpu_buffer_rsrc_t rs_km = mkrs(ws + O_KM);
#pragma unroll
      for (int kk = 0; kk < 3; ++kk) st16(rs_km, ko + (unsigned)(kk * 1024), (unsigned)s * KM_S + (unsigned)h * KM_H, kv[kk]);
      if (s + PRE < 63) st16(rs_p, eo, (unsigned)PSLOT(s + PRE), (u32x4){~0u, ~0u, ~0u, ~0u});
      SB();
      tb2 = (unsigned)__builtin_amdgcn_readfirstlane(tt.x);
      dur = min(dur + 1u, tb2 - lg0); *LP(volatile unsigned, fl0 + 96) = dur + (unsigned)a.d[3];
    }
    ADAPT(dly, spins, a.d[0]);
    if (W != 0) { u32x2 tt; LWAIT(tt, 104); tsv = (unsigned)__builtin_amdgcn_readfirstlane(tt.x); }
  }
#undef trk
}

constexpr int NSELF = RSELF;
__device__ __forceinline__ void f_part(const Args& a, unsigned char* lds, const int h, const int part, const int wave, const bool isr, const unsigned tag) {
  const int lane = lane_id(), l15 = lane & 15, lq = lane >> 4;
  const unsigned lb = lds_addr(lds);
  const int kk = 4 * part + wave;
  u32x4 af[2][6];
  {
    const unsigned vo = (unsigned)((8 * (l15 >> 2) + (l15 & 3)) * 12288 + lq * 16);
    const __amdgpu_buffer_rsrc_t rv = mkrs(a.Wv + 192 * h + (size_t)(32 * kk) * 6144);
#pragma unroll
    for (int t = 0; t < 2; ++t)
#pragma unroll
      for (int e = 0; e < 6; ++e) af[t][e] = __builtin_amdgcn_raw_buffer_load_b128(rv, (int)(vo + 64u * e), 4 * t * 12288, 0);
  }
  SB();
  unsigned dv; { const unsigned r = (unsigned)(8 * wave + (lane >> 3)); dv = r * 1280u + (((unsigned)lane & 7u) ^ (((r >> 1) & 1u) * 2u + ((r >> 3) & 1u) * 4u)) * 16u; PINV(dv); }
  const int g0 = 2 * part;
#define F_COL(g_) ((g_) + g0 < 10 ? (g_) + g0 : (g_) + g0 - 10)
#define F_DMA(g_, b_) do { const u16* s_ = a.Wo + (size_t)(192 * h) * 640 + 64 * F_COL(g_); _Pragma("unroll") for (int q = 0; q < 6; ++q) glds(s_ + q * (32 * 640), dv, lb + (b_) + (unsigned)((wave + 4 * q) * 1024)); } while (0)
#define F_RA(g_) (16384u + (unsigned)((g_) < 4 ? 3 - (g_) : ((g_) < 10 ? 9 - (g_) : 0)) * 24576u)
#pragma unroll 1
  for (int g = 0; g < FD0; ++g) F_DMA(g, F_RA(g));
  unsigned xv[5];
#pragma unroll
  for (int i = 0; i < 5; ++i) { const unsigned c = (unsigned)((wave + 4 * i) * 64 + lane), r = c / 80u; xv[i] = (c ^ r) * 16u + (r >> 2) * 5120u; PINV(xv[i]); }
#define F_XD(sg_, hf_, ad_) do { _Pragma("unroll") for (int i = 0; i < 5; ++i) glds(a.Bx + (size_t)(32 * (sg_) + 4 * (hf_)) * 640, xv[i], lb + (ad_) + (unsigned)(wave * 1024 + i * 4096)); } while (0)
  const __amdgpu_buffer_rsrc_t rs = mkrs(a.ws + O_FF + (size_t)((h >> 1) * 20 + kk) * 1024);
  unsigned vo = 0x80000000u, vo1 = (unsigned)((l15 >> 3) * (16 * 20 * 1024) + (h & 1) * 512 + lq * 128 + (l15 & 7) * 16);
  unsigned bo[4], bq[4];
  { const unsigned brow = (unsigned)(8 * lq + (l15 >> 2)), bg = ((brow >> 1) & 1u) * 2u + ((brow >> 3) & 1u) * 4u;
#pragma unroll
    for (int j = 0; j < 4; ++j) { bo[j] = lb + brow * 128u + (((unsigned)(2 * j + ((l15 & 3) >> 1))) ^ bg) * 16u + (unsigned)(l15 & 1) * 8u; PINV(bo[j]); bq[j] = bo[j] + F_RA(0); } }
#define F_TR(j_, n_) __builtin_bit_cast(u32x2, __builtin_amdgcn_ds_read_tr16_b64_v4i16((LAS bf16x4*)(bq[j_] + ((n_) >> 1) * 4096 + ((n_) & 1) * 512)))
  u32x2 bf[2][12]; f32x4 acc[2][2] = {{ZERO4, ZERO4}, {ZERO4, ZERO4}};
  if (FD0 == 2) VMW(6); else if (FD0 == 3) VMW(12); else if (FD0 == 4) VMW(18); else VMW(30);
  BAR();
#pragma unroll
  for (int n = 0; n < 12; ++n) bf[0][n] = F_TR(0, n);
  unsigned so = 0;
#pragma unroll 1
  for (int g = 0; g < 10; ++g) {
#pragma unroll
    for (int j = 0; j < 4; ++j) {
      unsigned w[4];
      if (g == 0 && j < 6 - FD0) F_DMA(FD0 + j, F_RA(FD0 + j));
      if (j == 3) { VMW(24); asm volatile("s_barrier" ::: "memory");
        if (g < 4) F_DMA(g + 6, F_RA(g));
        else if (g < 8 || (isr && NSELF > 2)) F_XD((isr ? 0 : NSELF) + ((g - 4) >> 1), ~g & 1, 143360u - (unsigned)(g - 4) * 20480u);
#pragma unroll
        for (int q = 0; q < 4; ++q) { bq[q] = bo[q] + F_RA(g + 1); PINV(bq[q]); }
        SB(); }
#pragma unroll
      for (int n = 0; n < 12; ++n) {
        const int e = n >> 1, t = n & 1;
        const u32x4 b = {bf[j & 1][2 * e].x, bf[j & 1][2 * e].y, bf[j & 1][2 * e + 1].x, bf[j & 1][2 * e + 1].y};
        if (e == 0) asm volatile("v_mfma_f32_16x16x32_bf16 %0, %1, %2, 0" : "=&v"(acc[j & 1][t]) : "v"(af[t][e]), "v"(b));
        else asm volatile("v_mfma_f32_16x16x32_bf16 %0, %1, %2, %0" : "+v"(acc[j & 1][t]) : "v"(af[t][e]), "v"(b));
        SB();
        bf[(j + 1) & 1][n] = F_TR((j + 1) & 3, n); SB();
        if (n >= 3 && n < 7) { const f32x4 c = acc[(j + 1) & 1][(n - 3) >> 1]; w[n - 3] = ((n - 3) & 1) ? cvtpk(c[2], c[3]) : cvtpk(c[0], c[1]); PINV(w[n - 3]); SB(); }
        if (n == 8) { st16(rs, vo, so, (u32x4){w[0], w[1], w[2], w[3]}); vo = vo1; so = (unsigned)(4 * F_COL(g) + j) * (2u * 16u * 20u * 1024u); SB(); }
      }
    }
  }
  asm volatile("s_nop 7" : "+v"(acc[1][0]), "+v"(acc[1][1]));
  st16(rs, vo, so, (u32x4){cvtpk(acc[1][0][0], acc[1][0][1]), cvtpk(acc[1][0][2], acc[1][0][3]), cvtpk(acc[1][1][0], acc[1][1][1]), cvtpk(acc[1][1][2], acc[1][1][3])});
  init_wait(a.ws, tag);
  VMW(0);
  BAR();
  if (wave == 0 && lane < 8) __hip_atomic_fetch_add((gu32*)((unsigned*)(a.ws + O_CNT) + CNT(C_FF + (h >> 3) * 8 + lane)), 1u, RLX_AGENT);
}
#define F_WAIT(rep_) do { wait_ge<FSL>(cnt + CNT(C_FF + 8 * wave + (rep_)), 40u, tmo); } while (0)
#define F_LD(t_, k_) ld16(rs_f, (unsigned)((l15 >> 3) * 512 + lq * 128 + (l15 & 7) * 16), (unsigned)(((t_) * 20 + (k_)) * 1024))
#define FOLD(o_, a_, b_) do { \
    const float f0_ = __int_as_float(__builtin_amdgcn_update_dpp(__float_as_int(o_[0]), __float_as_int(o_[0]), 0x128, 0xc, 0xf, false)), f1_ = __int_as_float(__builtin_amdgcn_update_dpp(__float_as_int(o_[1]), __float_as_int(o_[1]), 0x128, 0xc, 0xf, false)); \
    const float f2_ = __int_as_float(__builtin_amdgcn_update_dpp(__float_as_int(o_[2]), __float_as_int(o_[2]), 0x128, 0xc, 0xf, false)), f3_ = __int_as_float(__builtin_amdgcn_update_dpp(__float_as_int(o_[3]), __float_as_int(o_[3]), 0x128, 0xc, 0xf, false)); \
    a_ = t32(f0_, f2_); b_ = t32(f1_, f3_); } while (0)

constexpr int L_PH = 10240, L_RG = 16384, RG_SLOT = 20480, LAG = RLAG, NRS = LAG + 2;
template <int W>
__device__ __forceinline__ void r_loop(const Args& a, unsigned char* lds, const int u, const unsigned pol, const u32x8 (&F)[4][10]) {
  char* ws = a.ws; asm volatile("" : "+s"(ws));
  unsigned* const cnt = (unsigned*)(ws + O_CNT); unsigned* const tmo = cnt + CNT(C_TMO);
  char* const X1 = ws + O_X1; char* const P = ws + O_PS;
  const int lane = lane_id(), l15 = lane & 15, lq = lane >> 4;
  constexpr bool FIN = W == 0;
  constexpr int wave = W, NT = 4, t0 = 4 * W;
  constexpr int NP = W == 2 ? 16 : 20, NU = W == 1 ? NU1 : (W == 2 ? 8 : 8 - NU1), U0 = W == 1 ? 0 : (W == 2 ? NU1 : NU1 + 8), NL = NP + NU;
  const __amdgpu_buffer_rsrc_t rs_x1 = mkrs(X1), rs_p = mkrs(P);
  int ix = (l15 & 2) ? 0xEEEE : 0x4444; PINV(ix);
  unsigned pt0 = (unsigned)(((l15 >> 3) * 4 + lq) * 128 + (l15 & 7) * 16);
  unsigned pmo = (unsigned)(32768 + (2 * lq + (l15 >> 3)) * 128 + (l15 & 7) * 16);
  unsigned xvo = lane < 8 ? (unsigned)(lane * 16) : 0x80000000u;
  unsigned afb = (unsigned)(L_MEM + lane * 16);
  unsigned ovo = 0x80000000u;
  unsigned tub = (unsigned)(L_RG + lq * 1024 + l15 * 16), zwb = (unsigned)(L_RG + 16384 + t0 * 256 + l15 * 16 + lq * 4);
  unsigned ga = (unsigned)(L_MEM + (lq * 16 + ((lane >> 1) & 3) * 4 + (lane & 1)) * 16 + (lane & 8));
  unsigned oob = 0x80000000u; int tsa = (lane & 31) * 128;
  if (FIN) { PINV(pt0); PINV(pmo); PINV(xvo); PINV(oob); PINV(ga); PINV(tsa); } else { PINV(ovo); PINV(tub); }
  PINV(afb); PINV(zwb);
  const __amdgpu_buffer_rsrc_t rs_uo = mkrs(ws + O_UO + (size_t)u * U_U + U0 * 1024), rs_pt = mkrs(ws + O_PT - LAG * PT_SEG), rs_out = mkrs(a.out + 640 + 8 * u);
  const unsigned* const pop = cnt + CNT(C_PO + u); const unsigned* const tkp = cnt + CNT(C_TOK + u / 20);
  const __amdgpu_buffer_rsrc_t rs_po = mkrs(pop), rs_tk = mkrs(tkp - CNT(4) * (LAG - 1));
  int dlx = a.d[1] + 2 * a.d[4], dlp = a.d[2] + a.d[4];
  const unsigned pso0 = half_of(P, 0u, 0u, pol);
  const __amdgpu_buffer_rsrc_t rs_ts = mkrs(P + pso0 + TS_OFF);
#define TK_P(g_, c_, j_, so_) ld16(rs_pt, afb - L_MEM + (unsigned)((j_) * 1024), (so_) + (unsigned)PT_BLK(g_, c_))
#define PT_AT(i_) half_of(ws + O_PT - LAG * PT_SEG, (unsigned)(i_) * PT_SEG, 0u, pol)
#define TK_A(c_, j_) ((c_) < 4 ? *(const u32x4*)(lds + tua + (j_) * 4096 + (c_) * 256) : *(const u32x4*)(lds + tza + (j_) * 1024))
#define TK_C(n_) ((n_) < 4 ? 4 : ((n_) >> 2) - 1)
#define TK_MM(g_, b_, n0_, n1_, H_) do { u32x4 ta_[8]; \
      _Pragma("unroll") for (int n = (n0_); n < (n0_) + 8 && n < (n1_); ++n) ta_[n & 7] = TK_A(TK_C(n), n & 3); \
      SB(); \
      _Pragma("unroll") for (int n = (n0_); n < (n1_); ++n) { to = mfma(ta_[n & 7], tp[(b_) + n], n == 0 ? ZERO4 : to); asm volatile("" : "+a"(to)); \
        if (n + 8 < (n1_)) ta_[n & 7] = TK_A(TK_C(n + 8), n & 3); SB(); H_; } } while (0)
#define TK_PK(o_) do { float ra, rb; FOLD(to, ra, rb); u32x4 w; ROW16(w, cvtpk(ra, rb), k0_); o_ = (u32x4){w.x, w.z, w.y, w.w}; PINV(o_); } while (0)
#define TK_ST(g_, o_, v_) __builtin_amdgcn_raw_buffer_store_b128(o_, rs_out, (int)(v_), (int)(oso + (unsigned)((g_) * 20480)), 0)
#define P_A(i_) (*(const u32x4*)(lds + upa + (i_) * 1024))
#define PH_SET(k_) do { asm volatile("" ::: "memory"); *(volatile LAS unsigned*)(LAS unsigned char*)(lds + L_PH) = (unsigned)(3 * s + (k_)); asm volatile("" ::: "memory"); } while (0)
#define PH_WAIT(k_) do { unsigned n_ = 0; while ((unsigned)__builtin_amdgcn_readfirstlane(*(volatile LAS unsigned*)(LAS unsigned char*)(lds + L_PH)) < (unsigned)(3 * s + (k_)) && ++n_ < 4096u) __builtin_amdgcn_s_sleep(1); asm volatile("" ::: "memory"); } while (0)
#define P_PASS() do { \
        _Pragma("unroll") for (int i = 0; i < 16; ++i) { p[i] = ld16(rs_p, pt0 + 1024u * (i & 3), (i >> 2) == 0 ? pso : ((i >> 2) == 1 ? ps1 : ((i >> 2) == 2 ? ps2 : ps3))); SB(); } \
        _Pragma("unroll") for (int i = 0; i < 4; ++i) { p[16 + i] = ld16(rs_p, pmo + 1024u * i, pso); SB(); } \
        PH_SET(1); \
        _Pragma("unroll") for (int i = 0; i < NPA; ++i) up[i] = P_A(i); \
        SB(); \
        asm volatile("s_nop 1\n v_mfma_f32_16x16x32_bf16 %0, %1, %2, 0" : "=&{v[252:255]}"(o) : "v"(up[0]), "v"(p[0])); up[0] = P_A(NPA); SB(); \
        _Pragma("unroll") for (int i = 1; i < 19; ++i) { asm volatile("v_mfma_f32_16x16x32_bf16 %0, %1, %2, %0" : "+{v[252:255]}"(o) : "v"(up[i % NPA]), "v"(p[i])); if (i + NPA < 20) up[i] = P_A(i + NPA); SB(); } \
          \
        asm volatile("v_mfma_f32_16x16x32_bf16 %0, %3, %4, %0\n s_nop 7\n" \
                     "v_mov_b32_dpp v252, v252 row_ror:8 row_mask:0xc bank_mask:0xf\n v_mov_b32_dpp v254, v254 row_ror:8 row_mask:0xc bank_mask:0xf\n" \
                     "v_mov_b32_dpp v253, v253 row_ror:8 row_mask:0xc bank_mask:0xf\n v_mov_b32_dpp v255, v255 row_ror:8 row_mask:0xc bank_mask:0xf\n" \
                     "v_permlane32_swap_b32 v252, v254\n v_add_f32 v254, v252, v254\n v_permlane32_swap_b32 v253, v255\n v_add_f32 v255, v253, v255\n" \
                     "v_cmp_u_f32 %1, v254, v255\n v_cvt_pk_bf16_f32 v252, v254, v255\n v_cvt_pk_bf16_f32 v253, v254, v255\n v_cndmask_b32 %2, %5, %6, %1\n s_nop 0\n" \
                     "v_permlane16_swap_b32 v252, v253\n s_nop 1\n v_permlane32_swap_b32 v252, v254\n v_permlane32_swap_b32 v253, v255\n" \
                     "buffer_store_dwordx4 v[252:255], %2, %7, %8 offen sc1\n s_nop 1" \
                     : "+{v[252:255]}"(o), "=&s"(nan), "=&v"(xs) : "v"(up[19 % NPA]), "v"(p[19]), "v"(xvo), "v"(oob), "s"(rs_x1), "s"(xo_u) : "memory"); } while (0)
#define ROW16(w_, pk_, PK2_) do { unsigned k0_ = pk_; asm("" : "+v"(k0_)); float r2_ = ra; asm("" : "+v"(r2_)); const v2u_t c_ = __builtin_amdgcn_permlane16_swap(k0_, PK2_, false, false); \
        const v2u_t d_ = __builtin_amdgcn_permlane32_swap(c_[0], __float_as_uint(ra), false, false), e_ = __builtin_amdgcn_permlane32_swap(c_[1], __float_as_uint(rb), false, false); \
        w_ = (u32x4){d_[0], e_[0], d_[1], e_[1]}; } while (0)
  unsigned rc = 0, rn = RG_SLOT, ro = 2 * RG_SLOT;
  constexpr int NPA = 10;
  f32x4 to;
  u32x4 tp[NP], un[NU > 0 ? NU : 1];
#define R_Z(H_) do { u32x4 f[4]; f[0] = AFR(0); f[1] = AFR(1); f[2] = AFR(2); \
      _Pragma("unroll") for (int k = 0; k < 10; ++k) { \
        if (k + 3 < 10) f[(k + 3) % 4] = AFR(k + 3); \
        _Pragma("unroll") for (int t = 0; t < NT; ++t) { if (t < 3) asm volatile(SMI : "+v"(z[t]) : "v"(f[k % 4]), "a"(F[t][k]), "v"(ix)); else asm volatile(SMI : "+v"(z[t]) : "v"(f[k % 4]), "v"(F[t][k]), "v"(ix)); } \
        H_; SB(); } \
      asm volatile("s_nop 7" : "+v"(z[0]), "+v"(z[1]), "+v"(z[2]), "+v"(z[3])); \
      _Pragma("unroll") for (int t = 0; t < NT; ++t) *(unsigned*)(lds + zwa + t * 256) = cvtpk(nopk(z[t][0] + z[t][2]), nopk(z[t][1] + z[t][3])); \
      SB(); } while (0)
#define RING_ON() do { rc = rn; rn = rn == (NRS - 1) * RG_SLOT ? 0u : rn + RG_SLOT; ro = ro == (NRS - 1) * RG_SLOT ? 0u : ro + RG_SLOT; } while (0)
  if (FIN) {
    const unsigned xu = (unsigned)((u >> 6) * 16384 + (u & 63) * 128);
    unsigned xo = half_of(X1, 0u, 0u, pol), psp = 0;
    u32x4 gv[10];
#define R_GLD() do { unsigned xo2 = xo + 4096u, xo3 = xo + 16384u; _Pragma("unroll") for (int k = 0; k < 10; ++k) { gv[k] = ld16(rs_x1, afb - L_MEM + (unsigned)((k & 3) * 1024), k < 4 ? xo : (k < 8 ? xo2 : xo3)); SB(); } } while (0)
#define R_GST() do { m = 0u; _Pragma("unroll") for (int k = 0; k < 10; ++k) { *(u32x2*)(lds + gx + k * 1024) = (u32x2){gv[k].x, gv[k].y}; *(u32x2*)(lds + gx + k * 1024 + 32) = (u32x2){gv[k].z, gv[k].w}; m = max(m, max(gv[k].x, gv[k].z)); SB(); } } while (0)
#pragma unroll
    for (int k = 0; k < 10; ++k) gv[k] = (u32x4){0u, 0u, 0u, 0u};
    unsigned gx = ga + (unsigned)(L_RG + 4 * RG_SLOT);
#pragma unroll 1
    for (int s = 0;; ++s) {
      f32x4 z[NT];
#pragma unroll
      for (int t = 0; t < NT; ++t) { z[t] = ZERO4; PINV(z[t]); }
      unsigned zwa = zwb + rc, upa = afb + (unsigned)(L_RG - L_MEM) + rc; PINV(zwa); PINV(upa);
      unsigned pso = pso0 + (unsigned)PSLOT(s), xo_u = xo + X1_STEP + xu;
      unsigned ps1 = pso + 4096u, ps2 = pso + 16384u, ps3 = pso + 20480u; asm volatile("" : "+s"(pso), "+s"(ps1), "+s"(ps2), "+s"(ps3), "+s"(xo_u));
      ADAPT(dlp, psp, a.d[2]);
      RING_ON();
      st16(rs_x1, s + PRE + 1 < 64 ? xvo : oob, xo_u + PRE * X1_STEP, (u32x4){~0u, ~0u, ~0u, ~0u});
      SB();
      unsigned m, gsp = 0;
      R_GST();
      if (__builtin_expect(__ballot(m == ~0u) != 0ull, 0)) {
        for (;;) { R_GLD(); R_GST(); const bool go = spin_check(gsp, tmo); if (__ballot(m == ~0u) == 0ull || !go) break; }
      }
      const unsigned tsl = __builtin_amdgcn_raw_buffer_load_b32(rs_ts, tsa, PSLOT(s), 16); SB();
      BAR();
      R_Z((void)0);
      BAR();
      ADAPT(dlx, gsp, a.d[1]);
      unsigned tsv = ts_max(tsl);
      asm volatile("" : "+s"(tsv), "+s"(dlx));
      if (s == 63) break;
      if (__builtin_expect(tsv == ~0u, 0)) tsv = ts_wait(rs_ts, PSLOT(s));
      const unsigned lg0 = wait_rt(tsv + (unsigned)dlp);
      {
        u32x4 p[20], up[NPA], o; u64 nan; unsigned xs;
        P_PASS();
        SB();
        psp = 0;
        if (__builtin_expect((nan & 0x00ff00ff00ff00ffull) != 0ull, 0)) {

          for (;;) {
            P_PASS();
            unsigned bad = 0u;
#pragma unroll
            for (int i = 0; i < 20; ++i) bad |= (p[i].x | p[i].y) | (p[i].z | p[i].w);
            const bool go = spin_check(psp, tmo);
            if (__all((int)bad >= 0) || !go) break;
          }
          st16(rs_x1, xvo, xo_u, (u32x4){min(o.x, 0xfffffffeu), o.y, min(o.z, 0xfffffffeu), o.w});
        }
      }
      PH_SET(2);
      xo += X1_STEP; gx = ga;
      const unsigned tg = wait_rt(tsv + (unsigned)dlx);
      R_GLD();
      PH_SET(3);
    }
    ro = (63 - LAG) % NRS * RG_SLOT;
  } else {
#define R_NEXT(a_, b_, uso, pts) do { _Pragma("unroll") for (int n = (a_); n < (b_); ++n) { const int q = n - NP, n0 = n - 12; \
      if (q >= 0) un[q < 0 ? 0 : q] = ld16(rs_uo, afb - L_MEM + (unsigned)((q & 3) * 1024), uso + (unsigned)((q >> 2) * 4096)); \
      else if (W == 1 && n0 >= 0) tp[q < 0 ? n : 0] = TK_P(0, TK_C(n0), n0 & 3, pts); \
      else tp[q < 0 ? n : 0] = TK_P(W, TK_C(n), n & 3, pts); \
      SB(); } } while (0)
    u32x4 o1, o0; unsigned usp = U_S, ptp = PT_AT(0);
#pragma unroll
    for (int j = 0; j < NP; ++j) tp[j] = (u32x4){0u, 0u, 0u, 0u};
#pragma unroll
    for (int j = 0; j < NU; ++j) un[j] = (u32x4){0u, 0u, 0u, 0u};
#pragma unroll 1
    for (int s = 0;; ++s) {
      f32x4 z[NT];
#pragma unroll
      for (int t = 0; t < NT; ++t) { z[t] = ZERO4; PINV(z[t]); }
      unsigned tkl; u32x4 opl;
      unsigned zwa = zwb + rc, tua = tub + ro, tza = afb + (unsigned)(L_RG - L_MEM + 16384) + ro, uwa = afb + (unsigned)(L_RG - L_MEM + U0 * 1024) + rn; PINV(zwa); PINV(tua); PINV(tza); PINV(uwa);
      int sg = s - LAG; asm volatile("" : "+s"(sg));
      unsigned oso = (unsigned)(sg < 0 ? 0 : sg) * 81920u, uso = (unsigned)(s < 62 ? s + 2 : 63) * U_S, pts = PT_AT(s + 1); asm volatile("" : "+s"(oso), "+s"(uso), "+s"(pts));
      SB();
      BAR();
      R_Z(if (RN3 + k < NL) R_NEXT(RN3 + k, RN3 + k + 1, usp, ptp); if (k == TSK) { opl = ld16(rs_po, 0u, 0u); tkl = __builtin_amdgcn_raw_buffer_load_b32(rs_tk, 0, s * 512, 16); });
      BAR();
      const unsigned ov = ovo;
      if (W == 1) {
        u32x4 ta_[8]; f32x4 t1;
#define TK_C1(n_) TK_C((n_) < 12 ? (n_) : (n_) - 12)
#pragma unroll
        for (int n = 0; n < 8; ++n) ta_[n] = TK_A(TK_C1(n), n & 3);
        SB();
#pragma unroll
        for (int n = 0; n < 20; ++n) {
          if (n < 12) { t1 = mfma(ta_[n & 7], tp[n], n == 0 ? ZERO4 : t1); asm volatile("" : "+a"(t1)); } else { to = mfma(ta_[n & 7], tp[n], n == 12 ? ZERO4 : to); asm volatile("" : "+a"(to)); }
          if (n + 8 < 20) ta_[n & 7] = TK_A(TK_C1(n + 8), n & 3);
          SB(); }
        { float ra, rb, rc_, rd; FOLD(t1, ra, rb); FOLD(to, rc_, rd); u32x4 w; ROW16(w, cvtpk(ra, rb), k0_); o1 = (u32x4){w.x, w.z, w.y, w.w}; ra = rc_; rb = rd; ROW16(w, cvtpk(ra, rb), k0_); o0 = (u32x4){w.x, w.z, w.y, w.w};
          asm volatile("" : "+v"(o1), "+v"(o0)); }
      } else { TK_MM(W, 0, 0, 4 * W + 8, (void)0); TK_PK(o1); }
#define R_OUT() do { TK_ST(W, o1, ov); if (W == 1) TK_ST(0, o0, ov); SB(); } while (0)
      if (s + 1 >= NSELF) {
#pragma unroll
        for (int j = 0; j < NU; ++j) *(u32x4*)(lds + uwa + j * 1024) = un[j];
      }
      unsigned tok = (unsigned)__builtin_amdgcn_readfirstlane(tkl), oprog = (unsigned)__builtin_amdgcn_readfirstlane(min(min(opl.x, opl.y), min(opl.z, opl.w)));
      asm volatile("" : "+s"(tok), "+s"(oprog));
      if (s == 63) break;
      if (__builtin_expect(tok < 32u || oprog < (unsigned)(s + 3) || s < LAG, 0)) {
        if (s == LAG - 1) { const int ln = lane_id(); ovo = ln < 8 ? (unsigned)(ln * 2560) : 0x80000000u; }
        if (s >= LAG - 1 && tok < 32u) wait_ge<4>(tkp + CNT(4) * (s + 1 - LAG), 32u, tmo);
        if (s < 62 && s + 2 >= NSELF && oprog < (unsigned)(s + 3)) wait_u(pop, (unsigned)(s + 3), tmo);
      }
      const unsigned late = (unsigned)__builtin_amdgcn_readfirstlane(*(volatile LAS unsigned*)(LAS unsigned char*)(lds + L_PH)) > (unsigned)(3 * s) ? 1u : 0u;

      PH_WAIT(1 + late); R_NEXT(0, RN1, uso, pts);
      RING_ON();
      PH_WAIT(2 + late); R_NEXT(RN1, RN2, uso, pts);
      if (OPH == 2) R_OUT();
      PH_WAIT(3); R_NEXT(RN2, RN3, uso, pts); usp = uso; ptp = pts;
      if (OPH == 3) R_OUT();
    }
    { const unsigned ov = ovo, oso = (63 - LAG) * 81920u; R_OUT(); }
  }
  if (FIN) { const int ln = lane_id(); ovo = ln < 8 ? (unsigned)(ln * 2560) : 0x80000000u; }
#pragma unroll 1
  for (int sg = 64 - LAG; sg < 64; ++sg) {
    ro = ro == (NRS - 1) * RG_SLOT ? 0u : ro + RG_SLOT;
    wait_ge<1>(tkp + CNT(4) * sg, 32u, tmo);
#pragma unroll
    for (int n = 0; n < 4 * W + 8; ++n) tp[n] = TK_P(W, TK_C(n), n & 3, PT_AT(sg + LAG));
    unsigned tua = tub + ro, tza = afb + (unsigned)(L_RG - L_MEM + 16384) + ro; PINV(tua); PINV(tza);
    const unsigned oso = (unsigned)sg * 81920u;
    u32x4 o1; TK_MM(W, 0, 0, 4 * W + 8, (void)0); TK_PK(o1); TK_ST(W, o1, ovo);
  }
}

__device__ __forceinline__ void r_role(const Args& a, unsigned char* lds, const int u, const int wave) {
  char* ws = a.ws; asm volatile("" : "+s"(ws));
  unsigned* const cnt = (unsigned*)(ws + O_CNT); unsigned* const tmo = cnt + CNT(C_TMO);
  const int lane = lane_id(), l15 = lane & 15, lq = lane >> 4;
  if (wave == 0) *(u32x4*)(lds + L_PH) = (u32x4){0u, 0u, 0u, 0u};
  unsigned xq[2][4];
#pragma unroll
  for (int j = 0; j < 4; ++j) { xq[0][j] = lds_addr(lds) + (unsigned)(40960 + l15 * 1280 + ((4 * j + lq) ^ l15) * 16); xq[1][j] = xq[0][j] + 81920u; PINV(xq[0][j]); PINV(xq[1][j]); }
  if (wave < 2) {
#pragma unroll
    for (int k = 5 * wave, n = 0; n < 5; ++k, ++n) { const u32x4 v = ld16(mkrs(ws + O_X1), (unsigned)lane * 16u, (unsigned)((k & 7) * 1024 + (k >> 3) * 16384));
      unsigned char* d = lds + L_MEM + k * 1024 + (lq * 16 + ((lane >> 1) & 3) * 4 + (lane & 1)) * 16 + (lane & 8); *(u32x2*)d = (u32x2){v.x, v.y}; *(u32x2*)(d + 32) = (u32x2){v.z, v.w}; }
  }
  const unsigned pol = wait_ge<8>(cnt + CNT(C_POL), 1u, tmo) & 1u;
  F_WAIT(u / 20);
  const __amdgpu_buffer_rsrc_t rs_f = mkrs(ws + O_FF + (size_t)(u * 16 + 4 * wave) * 20480);
  u32x4 Fh[4][20]; f32x4 ua[2 * NSELF][4]; u32x4 xa[2 * NSELF];
  constexpr int FW = RFW;
#pragma unroll
  for (int k = 0; k < FW; ++k)
#pragma unroll
    for (int t = 0; t < 4; ++t) { Fh[t][k] = F_LD(t, k); SB(); }
#define R_XF(i_, k_) (*(const LAS u32x4*)(xq[(i_) < 2 ? 1 : 0][(k_) & 3] + ((i_) < 4 ? (i_) : (i_) - 4) * 20480 + ((k_) >> 2) * 256))
#pragma unroll
  for (int i = 0; i < 2 * NSELF; ++i) xa[i] = R_XF(i, 0);
#pragma unroll
  for (int k = 0; k < 20; ++k) {
#pragma unroll
    for (int t = 0; t < 4; ++t) {
#pragma unroll
      for (int i = 0; i < 2 * NSELF; ++i) {
        if (k == 0) { if (t < 3) asm volatile("v_mfma_f32_16x16x32_bf16 %0, %1, %2, 0" : "=&v"(ua[i][t]) : "v"(xa[i]), "a"(Fh[t][k])); else asm volatile("v_mfma_f32_16x16x32_bf16 %0, %1, %2, 0" : "=&v"(ua[i][t]) : "v"(xa[i]), "v"(Fh[t][k])); }
        else { if (t < 3) asm volatile("v_mfma_f32_16x16x32_bf16 %0, %1, %2, %0" : "+v"(ua[i][t]) : "v"(xa[i]), "a"(Fh[t][k])); else asm volatile("v_mfma_f32_16x16x32_bf16 %0, %1, %2, %0" : "+v"(ua[i][t]) : "v"(xa[i]), "v"(Fh[t][k])); }
        if (t == 3 && k + 1 < 20) xa[i] = R_XF(i, k + 1);
        SB(); }
      if (k + FW < 20) { Fh[t][k + FW] = F_LD(t, k + FW); SB(); }
    }
  }
  asm volatile("s_nop 7" : "+v"(ua[2 * NSELF - 3][3]), "+v"(ua[2 * NSELF - 2][3]), "+v"(ua[2 * NSELF - 1][3]));
  BAR();
#pragma unroll
  for (int g = 0; g < NSELF; ++g)
#pragma unroll
    for (int t = 0; t < 4; ++t)
      *(u32x4*)(lds + L_RG + g * RG_SLOT + (4 * wave + t) * 1024 + lane * 16) = (u32x4){cvtpk(ua[2 * g][t][0], ua[2 * g][t][1]), cvtpk(ua[2 * g][t][2], ua[2 * g][t][3]), cvtpk(ua[2 * g + 1][t][0], ua[2 * g + 1][t][1]), cvtpk(ua[2 * g + 1][t][2], ua[2 * g + 1][t][3])};
  u32x8 F[4][10];
#pragma unroll
  for (int t = 0; t < 4; ++t)
#pragma unroll
    for (int k = 0; k < 10; ++k) F[t][k] = cat8(Fh[t][2 * k], Fh[t][2 * k + 1]);
  if (wave == 0) r_loop<0>(a, lds, u, pol, F); else if (wave == 1) r_loop<1>(a, lds, u, pol, F); else if (wave == 2) r_loop<2>(a, lds, u, pol, F); else r_loop<3>(a, lds, u, pol, F);
}

template <bool QK>
__device__ __forceinline__ void g_role(const Args& a, unsigned char* lds, const int u, const int wave, const unsigned tag) {
  char* ws = a.ws; asm volatile("" : "+s"(ws));
  unsigned* const cnt = (unsigned*)(ws + O_CNT); unsigned* const tmo = cnt + CNT(C_TMO);
  const int lane = lane_id(), l15 = lane & 15, lq = lane >> 4;
  u32x8 F[4][10];
  const unsigned rot = QK ? (unsigned)u % 5u : 0u;
  unsigned dvo[10];
#pragma unroll
  for (int i = 0; i < 10; ++i) { const unsigned c = (unsigned)((wave + 4 * i) * 64 + lane), r = c / 80u, pc = c - r * 80u, g = (pc >> 4) + rot;
    dvo[i] = (QK ? r : 8u * ((r & 15u) >> 2) + 4u * (r >> 4) + (r & 3u)) * 1280u + ((g < 5u ? g : g - 5u) * 16u + ((pc ^ r) & 15u)) * 16u; PINV(dvo[i]); }
#define G_XD1(sg_, b_, i_) glds(a.Bx + (size_t)(32 * ((sg_) < 64 ? (sg_) : 63)) * 640, dvo[i_], lds_addr(lds) + (b_) + (unsigned)(wave * 1024 + (i_) * 4096))
#define G_XD(sg_, b_) do { _Pragma("unroll") for (int xi_ = 0; xi_ < 10; ++xi_) G_XD1(sg_, b_, xi_); } while (0)
  if (QK) {
    const char* W_ = (const char*)((u < 24 ? a.Wk : a.Wq) + 256 * (u < 24 ? u : u - 24));
    unsigned svo[7];
#pragma unroll
    for (int q = 0; q < 7; ++q) { const unsigned c = (unsigned)((wave + 4 * q) * 64 + lane), r = c >> 5, pc = c & 31u; svo[q] = r * 12288u + 16u * (pc ^ (((r & 3u) | ((r >> 1) & 4u)) << 1)); PINV(svo[q]); }
    unsigned sv7; { const unsigned c = (unsigned)((wave + 28) * 64 + lane), r = c >> 5, pc = c & 31u; sv7 = r * 12288u + 16u * (pc ^ (((r & 3u) | ((r >> 1) & 4u)) << 1)); PINV(sv7); }
#define G_WD(i_) do { const unsigned g_ = (unsigned)((i_) >> 1) + rot; const char* s_ = W_ + (size_t)((g_ < 5u ? g_ : g_ - 5u) * 128u + ((i_) & 1) * 64u) * 12288; \
      _Pragma("unroll") for (int q = 0; q < 8; ++q) glds(s_, q < 7 ? svo[q < 7 ? q : 0] : sv7, lds_addr(lds) + (unsigned)(((i_) % 3) * 32768 + (wave + 4 * q) * 1024)); SB(); } while (0)
    G_WD(0); G_WD(1); G_XD(0, 122880u); G_WD(2);
    const unsigned row = (unsigned)(8 * lq + (l15 >> 2)), sw = ((row & 3u) | ((row >> 1) & 4u)) << 1;
    unsigned tro[4];
#pragma unroll
    for (int t = 0; t < 4; ++t) { tro[t] = row * 512u + (((unsigned)(2 * (4 * wave + t) + ((l15 & 3) >> 1))) ^ sw) * 16u + (unsigned)(l15 & 1) * 8u; PINV(tro[t]); }
#pragma unroll
    for (int i = 0; i < 10; ++i) {
      if (i == 0) VMW(26); else if (i == 1) VMW(26); else if (i < 8) VMW(16); else if (i == 8) VMW(8); else { init_wait(ws, tag); VMW(0); }
      BAR();
#pragma unroll
      for (int t = 0; t < 4; ++t) F[t][i] = cat8(trrd2(lds, tro[t] + (unsigned)((i % 3) * 32768), 2048u), trrd2(lds, tro[t] + (unsigned)((i % 3) * 32768 + 16384), 2048u));
      BAR();
      if (i + 3 < 10) G_WD(i + 3);
    }
    G_XD(1, 81920u);
  } else {
    F_WAIT(4 + u / 20);
  }
  u32x4 Wf[4][20];
  const __amdgpu_buffer_rsrc_t rs_f = mkrs(ws + O_FF + (size_t)(u * 16 + 4 * wave) * 20480);
#pragma unroll
  for (int k = 0; k < 20; ++k)
#pragma unroll
    for (int t = 0; t < 4; ++t) {
      if (QK) { Wf[t][k] = (k & 1) ? hi4(F[t][k >> 1]) : lo4(F[t][k >> 1]); if (t < 3) asm volatile("" : "+a"(Wf[t][k])); else PINV(Wf[t][k]); }
      else if (k < GFW) { Wf[t][k] = F_LD(t, k); SB(); } }
  const int gw = 4 * (u < 24 ? u : u - 24) + wave, hh = gw / 3, k3 = gw - 3 * hh;
  const __amdgpu_buffer_rsrc_t rs_fl = mkrs(QK ? cnt + CNT(C_PT_ + hh) + (u < 24 ? 0 : 4) + k3 : cnt + CNT(C_PO + u) + wave);
  const __amdgpu_buffer_rsrc_t rs_o = mkrs(QK ? ws + (u < 24 ? O_KF : O_QF) + (size_t)hh * QK_H + k3 * 2048 : ws + O_UO + (size_t)u * U_U + wave * 4096);
  int flo = lane == 0 ? 0 : (int)0x80000000u; PINV(flo);
  constexpr int I0 = QK ? 0 : NSELF;
  unsigned bc = 122880u, bn = 40960u;
  const unsigned lb = lds_addr(lds);
  unsigned xr[4], xq[4], vo = 0x80000000u, fa = lb; PINV(fa);
#pragma unroll
  for (int q = 0; q < 4; ++q) { xr[q] = lb + (unsigned)(l15 * 1280 + ((4 * q + lq) ^ l15) * 16); PINV(xr[q]); xq[q] = xr[q] + bc; }
#define G_XF(rt_, k_) (*(const LAS u32x4*)(xq[(k_) & 3] + (rt_) * 20480 + ((k_) >> 2) * 256))
#define G_MI(bc_, c_, d_, x_, w_) do { if (QK) asm volatile("v_mfma_f32_16x16x32_bf16 %0, %2, %1, " c_ : "+v"(d_) : "v"(x_), bc_(w_)); else asm volatile("v_mfma_f32_16x16x32_bf16 %0, %1, %2, " c_ : "+v"(d_) : "v"(x_), bc_(w_)); } while (0)
#define G_M(k_, t_, rt_) do { if ((t_) < 3) { if ((k_) == 0) G_MI("a", "0", acc[rt_][t_], xa[(k_) & 3][rt_], Wf[t_][k_]); else G_MI("a", "%0", acc[rt_][t_], xa[(k_) & 3][rt_], Wf[t_][k_]); } \
      else { if ((k_) == 0) G_MI("v", "0", acc[rt_][t_], xa[(k_) & 3][rt_], Wf[t_][k_]); else G_MI("v", "%0", acc[rt_][t_], xa[(k_) & 3][rt_], Wf[t_][k_]); } SB(); } while (0)
#define G_PK(c_) do { const f32x4 v_ = acc[((c_) >> 1) & 1][(c_) >> 2]; const int sj = QK ? ((c_) & 2) + ((c_) >> 3) : (c_) >> 2, se = QK ? (((c_) >> 1) & 2) + ((c_) & 1) : (c_) & 3; \
      pk[sj][se] = ((c_) & 1) ? cvtpk(v_[2], v_[3]) : cvtpk(v_[0], v_[1]); PINV(pk[sj]); SB(); } while (0)
#define G_ST(j_, i_) do { \
      st16(rs_o, vo, (unsigned)(i_) * (QK ? QK_S : U_S) + (unsigned)(QK ? ((j_) >> 1) * 6144 + ((j_) & 1) * 1024 : (j_) * 1024), pk[j_]); SB(); } while (0)
  f32x4 acc[2][4]; u32x4 xa[4][2], pk[4]; unsigned m0k, fw = lb + (unsigned)wave * 4u; PINV(fw);
#pragma unroll
  for (int j = 0; j < 8; ++j) { acc[j >> 2][j & 3] = ZERO4; PINV(acc[j >> 2][j & 3]); }
#pragma unroll
  for (int c = 0; c < 4; ++c) pk[c] = (u32x4){0u, 0u, 0u, 0u};
  *(volatile LAS unsigned*)fw = 0u;
  if (QK) VMW(10);
  BAR();
  asm volatile("s_mov_b32 %0, m0" : "=s"(m0k));
#pragma unroll
  for (int k = 0; k < 2; ++k) { xa[k][0] = G_XF(0, k); xa[k][1] = G_XF(1, k); }
#define G_K(FIRST_, k_) { constexpr int k = k_; \
      _Pragma("unroll") for (int j = 0; j < 8; ++j) { \
        const int t = j >> 1, rt = j & 1, gp = k == 19 ? j : (k == 0 ? 8 + j : -1), dq = 2 * (k - 12) + (j == 6); const bool dm = k >= 12 && k < 17 && (j == 3 || j == 6); \
        if (dm) asm volatile("s_mov_b32 m0, %0" :: "s"(md + (unsigned)(dq * 4096))); \
        G_M(k, t, rt); \
        if (FIRST_ && rt == 1 && k + GFW < 20) { Wf[t][k + GFW < 20 ? k + GFW : 0] = F_LD(t, k + GFW); SB(); } \
        if (j < 2) { xa[(k + 2) & 3][j] = G_XF(j, (k + 2) % 20); SB(); } \
        _Pragma("unroll") for (int c = 0; c < 16; ++c) if (gp == 3 + c * 3 / 4) G_PK(c); \
        if (k % 3 == 1 && k < 12 && j == 5) G_ST(k / 3, ip);        \
        if (k == 2 && j == 3) { xo_ = (unsigned)min(i + 2, 63) * 40960u; asm volatile("" : "+s"(xo_)); SB(); } \
        if (k == 2 && j == 4) { xs = (const u16*)((const char*)a.Bx + xo_); asm volatile("" : "+s"(xs)); SB(); } \
        if (k == 2 && j == 5) { md = lb + bn + (unsigned)wave * 1024u; asm volatile("" : "+s"(md)); bn = bc; SB(); } \
        if (k == 2 && j == 6) { bc = bc == 40960u ? 122880u : bc - 40960u; asm volatile("" : "+s"(bc)); SB(); } \
        if (k == 3 && j == 3) VV(iv, (unsigned)(i + 1)); \
        if (k == 3 && j == 5) VV(iw, (unsigned)i); \
        if (k == 6 && j == 3) { if (FIRST_) ; else VMW(2); *(volatile LAS unsigned*)fw = iv; SB(); } \
        if (k == 10 && j == 3) { fg = *(const volatile LAS u32x4*)fa; SB(); } \
        if (k == 11 && j == 2) VV(mn, min(fg.x, fg.y)); \
        if (k == 11 && j == 3) VV(mn, min3u(mn, fg.z, fg.w)); \
        if (k == 11 && j == 4) { \
          if (__builtin_expect(__ballot(mn < iv) != 0ull, 0)) { int n_ = 0; do { fg = *(const volatile LAS u32x4*)fa; mn = min(min3u(fg.x, fg.y, fg.z), fg.w); } while (__ballot(mn < iv) != 0ull && ++n_ < (1 << 22)); } \
          asm volatile("" ::: "memory"); SB(); } \
        if (dm) { asm volatile("global_load_lds_dwordx4 %0, %1" :: "v"(dvo[dm ? dq : 0]), "s"(xs) : "memory"); SB(); } \
        if (k == 17 && j == 2) { VMW(10); __builtin_amdgcn_raw_buffer_store_b32(iw, rs_fl, flo, 0, 16); SB(); }        \
        if (k == 17 && j >= 4) { xq[j - 4] = xr[j - 4] + bc; PINV(xq[j - 4]); SB(); } \
      } }
#define G_K4(F_, k_) G_K(F_, k_) G_K(F_, k_ + 1) G_K(F_, k_ + 2) G_K(F_, k_ + 3)
#define G_SEG(F_) do { const u16* xs; unsigned xo_, md, iv, iw, mn; u32x4 fg; G_K4(F_, 0) G_K4(F_, 4) G_K4(F_, 8) G_K4(F_, 12) G_K4(F_, 16) } while (0)
#define G_FLUSH(i_) do { asm volatile("s_nop 7" : "+v"(acc[1][1]), "+v"(acc[0][2]), "+v"(acc[1][2]), "+v"(acc[0][3]), "+v"(acc[1][3])); \
      _Pragma("unroll") for (int c = 7; c < 16; ++c) G_PK(c); \
      _Pragma("unroll") for (int j = 0; j < 4; ++j) G_ST(j, i_); } while (0)
#define G_FIRST() do { vo = (unsigned)lane_id() * 16u; G_FLUSH(I0); VMW(0); __builtin_amdgcn_raw_buffer_store_b32(I0 + 1u, rs_fl, flo, 0, 16); } while (0)
  if (!QK) { const int i = I0, ip = I0; G_SEG(true); G_FIRST(); }
#pragma unroll 1
  for (int i = QK ? I0 : I0 + 1, ip = I0; i < 64; ip = i, ++i) {
    G_SEG(false);
    if (__builtin_expect(i < I0 + (QK ? AQ0 + APN : AU0 + APU), 0)) {
      if (i == I0) G_FIRST();
      else if (i >= I0 + (QK ? AQ0 : AU0)) {
        const int n = ((QK ? (i - AQ0) * 48 : APN * 48 + (i - I0 - AU0) * 80) + u) * 32768 + wave * 8192;
        const char* src = n < 2457600 ? (const char*)a.Awqkv + n : (n < 3276800 ? (const char*)a.Awo + (n - 2457600) : (const char*)a.Ax + (n < 5898240 ? n - 3276800 : 0));
        touch(src + lane_id() * 128, lb + 256u); }
    }
  }
  G_FLUSH(63);
  asm volatile("s_mov_b32 m0, %0" :: "s"(m0k));
  VMW(0);
  __builtin_amdgcn_raw_buffer_store_b32(64u, rs_fl, flo, 0, 16);
  BAR();
}

__device__ __forceinline__ void att_role(const Args& a, const int h, const int par) {
  char* ws = a.ws;
  unsigned* const cnt = (unsigned*)(ws + O_CNT); unsigned* const tmo = cnt + CNT(C_TMO);
  const int lane = lane_id(), l15 = lane & 15, lq = lane >> 4;
  const __amdgpu_buffer_rsrc_t rs_q = mkrs(ws + O_QF + (size_t)h * QK_H), rs_k = mkrs(ws + O_KF + (size_t)h * QK_H), rs_km = mkrs(ws + O_KM + (size_t)h * KM_H), rs_pt = mkrs(ws + O_PT + (size_t)h * 128);
  const unsigned pol = wait_ge<8>(cnt + CNT(C_POL), 1u, tmo) & 1u;
  unsigned pv[5];
  { const int gl = l15 >> 3, cl = lq >> 1; const unsigned ro = (unsigned)((l15 & 7) * 16 + (lq & 1) * 8);
    pv[0] = cl <= gl ? (unsigned)PT_BLK(gl, cl) + ro : 0x80000000u; pv[1] = (unsigned)PT_BLK(2 + gl, cl) + ro; pv[2] = cl <= gl ? (unsigned)PT_BLK(2 + gl, 2 + cl) + ro : 0x80000000u;
    pv[3] = lq < 2 ? (unsigned)PT_BLK(gl, 4) + ro : 0x80000000u; pv[4] = lq < 2 ? (unsigned)PT_BLK(2 + gl, 4) + ro : 0x80000000u; }
  const float scale = 0.07216878364870323f, LOG2E = 1.4426950408889634f;
  unsigned tprog = 0;
  u32x4 qn[12], kn[12];
#define AT_LD(s_) do { if (tprog < (unsigned)((s_) + 1)) tprog = wait_t<16>(cnt + CNT(C_PT_ + h), true, (unsigned)((s_) + 1), tmo); \
    _Pragma("unroll") for (int i = 0; i < 12; ++i) { kn[i] = ld16(rs_k, (unsigned)(i * 1024 + lane * 16), (unsigned)(s_) * QK_S); qn[i] = ld16(rs_q, (unsigned)(i * 1024 + lane * 16), (unsigned)(s_) * QK_S); } SB(); } while (0)
  AT_LD(par);
#pragma unroll 1
  for (int s = par; s < 64; s += 2) {
    f32x4 sc[2][3];
    u32x4 qf[2][6];
    float mx[2], sum[2];
#pragma unroll
    for (int q = 0; q < 2; ++q)
#pragma unroll
      for (int t = 0; t <= q; ++t) {
        sc[q][t] = ZERO4;
#pragma unroll
        for (int kk = 0; kk < 6; ++kk) sc[q][t] = mfma(kn[6 * t + kk], qn[6 * q + kk], sc[q][t]);
      }
#pragma unroll
    for (int i = 0; i < 12; ++i) { qf[i / 6][i % 6] = qn[i]; PINV(qf[i / 6][i % 6]); }
    asm volatile("" : "+a"(sc[0][0]), "+a"(sc[1][0]), "+a"(sc[1][1])); SB();
    AT_LD(s < 62 ? s + 2 : s);
#pragma unroll
    for (int q = 0; q < 2; ++q) {
      mx[q] = -3.0e38f;
#pragma unroll
      for (int t = 0; t <= q; ++t)
#pragma unroll
        for (int r = 0; r < 4; ++r) {
          const float v = rbf(rbf(sc[q][t][r]) * scale);
          sc[q][t][r] = (t < q || 4 * lq + r <= l15) ? v : -3.0e38f;
          mx[q] = fmaxf(mx[q], sc[q][t][r]);
        }
      mx[q] = max32(max16(mx[q]));
      sum[q] = 0.f;
#pragma unroll
      for (int t = 0; t <= q; ++t)
#pragma unroll
        for (int r = 0; r < 4; ++r) { const float e = (t < q || 4 * lq + r <= l15) ? __builtin_amdgcn_exp2f(nopk(sc[q][t][r] - mx[q]) * LOG2E) : 0.f; sc[q][t][r] = e; sum[q] += e; }
      PINV(mx[q]); PINV(sum[q]); PINV(sc[q][0]);
    }
    PINV(sc[1][1]);
    SB();
    if (s >= 60) wait_ge<2>(cnt + CNT(C_PS + h), (unsigned)(s == 63 ? 63 : s + 1), tmo); else wait_ge<6>(cnt + CNT(C_PS + h), (unsigned)(s + 1), tmo);
    {
      u32x4 km[6];
      unsigned spins = 0;
      for (;;) {
        unsigned m = 0u;
#pragma unroll
        for (int kk = 0; kk < 6; ++kk) { km[kk] = ld16(rs_km, (unsigned)(kk * 512 + lq * 128 + (l15 & 7) * 16), (unsigned)s * KM_S); m = max(m, max(km[kk].x, km[kk].z)); }
        if (__all(m != ~0u)) break;
        __builtin_amdgcn_s_sleep(3);
        if (!spin_check(spins, tmo)) break;
      }
#pragma unroll
      for (int q = 0; q < 2; ++q) {
        sc[q][2] = ZERO4;
#pragma unroll
        for (int kk = 0; kk < 6; ++kk) sc[q][2] = mfma(km[kk], qf[q][kk], sc[q][2]);
      }
    }
    const unsigned po = half_of(ws + O_PT, (unsigned)s * PT_SEG, 0u, pol);
#pragma unroll
    for (int q = 0; q < 2; ++q) {
      float m = mx[q];
#pragma unroll
      for (int r = 0; r < 4; ++r) { sc[q][2][r] = rbf(rbf(sc[q][2][r]) * scale); m = fmaxf(m, sc[q][2][r]); }
      m = max16(m);
      const float f = __builtin_amdgcn_exp2f(nopk(mx[q] - m) * LOG2E);
      float sm = 0.f;
#pragma unroll
      for (int r = 0; r < 4; ++r) { sc[q][2][r] = __builtin_amdgcn_exp2f(nopk(sc[q][2][r] - m) * LOG2E); sm += sc[q][2][r]; }
      const float inv = __builtin_amdgcn_rcpf(sum32(sum16(__builtin_fmaf(sm, 0.5f, sum[q] * f)))), gg = f * inv;
      st8(rs_pt, pv[q], po, cvtpk(sc[q][0][0] * gg, sc[q][0][1] * gg), cvtpk(sc[q][0][2] * gg, sc[q][0][3] * gg));
      if (q == 1) st8(rs_pt, pv[2], po, cvtpk(sc[1][1][0] * gg, sc[1][1][1] * gg), cvtpk(sc[1][1][2] * gg, sc[1][1][3] * gg));
      st8(rs_pt, pv[3 + q], po, cvtpk(sc[q][2][0] * inv, sc[q][2][1] * inv), cvtpk(sc[q][2][2] * inv, sc[q][2][3] * inv));
    }
    VMW(0);
    if (lane < 4) __hip_atomic_fetch_add((gu32*)(cnt + CNT(C_TOK + 4 * s + lane)), 1u, RLX_AGENT);
  }
}

__device__ __forceinline__ float dot2bf(unsigned x, unsigned y, float c) { return __builtin_amdgcn_fdot2_f32_bf16(__builtin_bit_cast(bf16x2_t, x), __builtin_bit_cast(bf16x2_t, y), c, false); }
__device__ __forceinline__ void a_role(const Args& a, unsigned char* lds, const int j, const int rg, const int wave) {
  char* ws = a.ws; asm volatile("" : "+s"(ws));
  unsigned* const cnt = (unsigned*)(ws + O_CNT); unsigned* const tmo = cnt + CNT(C_TMO);
  const int lane = lane_id(), l15 = lane & 15, lq = lane >> 4;
  const unsigned lb = lds_addr(lds);
  constexpr int LR = 81920, RS = 288;
  const int r0 = 256 * rg - 16;
  {
    u32x4 wt[2][20];
    {
      const unsigned r = (unsigned)(4 * wave + lq), p0 = (unsigned)l15 ^ (((r & 3u) | ((r >> 1) & 4u)) << 1), p = p0 < 15u ? p0 : 14u;
      unsigned wv = r * 3840u + (p / 5u) * 1280u + (p % 5u) * 16u; PINV(wv);
#pragma unroll
      for (int q = 0; q < 40; ++q) glds(a.Awqkv + 40 * j + q * (16 * 1920), wv, lb + (unsigned)((wave + 4 * q) * 1024));
    }
    unsigned dvo[5];
#pragma unroll
    for (int i = 0; i < 5; ++i) { const unsigned c = (unsigned)((wave + 4 * i) * 64 + lane), r = c / 80u; dvo[i] = (c ^ r) * 16u; PINV(dvo[i]); }
#define A_XS(t_) (a.Ax + (size_t)max(r0 + 16 * min(t_, 16), 0) * 640)
#define A_XD1(src_, t_, i_) glds(src_, dvo[i_], lb + (unsigned)(((t_) & 3) * 20480 + wave * 1024 + (i_) * 4096))
    const unsigned row = (unsigned)(8 * lq + (l15 >> 2)), sw = ((row & 3u) | ((row >> 1) & 4u)) << 1;
    unsigned tro[2];
#pragma unroll
    for (int t = 0; t < 2; ++t) tro[t] = row * 256u + (((unsigned)(2 * (2 * wave + t) + ((l15 & 3) >> 1))) ^ sw) * 16u + (unsigned)(l15 & 1) * 8u;
#pragma unroll
    for (int i = 0; i < 5; ++i) {
      if (i == 0) VMW(32); else if (i == 1) VMW(24); else if (i == 2) VMW(16); else if (i == 3) VMW(8); else VMW(15);
      BAR();
      if (i == 3) {
#pragma unroll
        for (int q = 0; q < 15; ++q) A_XD1(A_XS(q / 5), q / 5, q % 5);
      }
#pragma unroll
      for (int t = 0; t < 2; ++t) { unsigned o_ = tro[t] + (unsigned)(i * 32768); PINV(o_);
#pragma unroll
        for (int k = 0; k < 4; ++k) wt[t][4 * i + k] = trrd2(lds, o_ + (unsigned)(k * 8192), 1024u); }
      SB();
    }
    unsigned xr[4], xq[4], wo_[2];
#pragma unroll
    for (int q = 0; q < 4; ++q) { xr[q] = lb + (unsigned)(l15 * 1280 + ((4 * q + lq) ^ l15) * 16); PINV(xr[q]); xq[q] = xr[q]; }
#pragma unroll
    for (int t = 0; t < 2; ++t) { const int v = 8 * wave + 4 * t + lq; wo_[t] = lb + (unsigned)(LR + l15 * RS + (v < 30 ? (v / 5) * 48 + (v % 5) * 8 : (v - 30) * 48 + 40)); }
    unsigned fa = lb + 163824u, fw = fa + (unsigned)wave * 4u; PINV(fa); PINV(fw);
    BAR();
    *(volatile LAS unsigned*)fw = 0u;
    VMW(10);
    BAR();
#define A_XF(k_) (*(const LAS u32x4*)(xq[(k_) & 3] + ((k_) >> 2) * 256))
    f32x4 acc[2][2] = {{ZERO4, ZERO4}, {ZERO4, ZERO4}}; u32x4 xa[4];
#pragma unroll
    for (int k = 0; k < 3; ++k) xa[k] = A_XF(k);
#define A_TILE(T_, P_) do { const u16* xs = A_XS((T_) + 3); unsigned md = lb + (unsigned)((((T_) + 3) & 3) * 20480 + wave * 1024), iv, mn; asm volatile("" : "+s"(xs)); asm volatile("" : "+s"(md)); u32x2 pw[2]; u32x4 fg; \
      _Pragma("unroll") for (int k = 0; k < 20; ++k) { \
        _Pragma("unroll") for (int t = 0; t < 2; ++t) { const bool dm = t == 1 && k >= 11 && k < 16; \
          if (dm) asm volatile("s_mov_b32 m0, %0" :: "s"(md + (unsigned)((k - 11) * 4096))); \
          if (k == 0) asm volatile("v_mfma_f32_16x16x32_bf16 %0, %1, %2, 0" : "=&v"(acc[P_][t]) : "v"(wt[t][k]), "v"(xa[k & 3])); \
          else asm volatile("v_mfma_f32_16x16x32_bf16 %0, %1, %2, %0" : "+v"(acc[P_][t]) : "v"(wt[t][k]), "v"(xa[k & 3])); \
          SB(); \
          if (t == 0) { xa[(k + 3) & 3] = A_XF((k + 3) % 20); SB(); } \
          if (t == 1 && k == 0) VV(iv, (unsigned)((T_) + 1)); \
          if (t == 1 && k == 1) { VMW(5); *(volatile LAS unsigned*)fw = iv; SB(); }        \
          if (t == 1 && (k == 2 || k == 3)) { pw[k & 1] = (u32x2){cvtpk(acc[1 - (P_)][k & 1][0], acc[1 - (P_)][k & 1][1]), cvtpk(acc[1 - (P_)][k & 1][2], acc[1 - (P_)][k & 1][3])}; PINV(pw[k & 1]); SB(); } \
          if (t == 1 && k == 4) { *(LAS u32x2*)wo_[0] = pw[0]; *(LAS u32x2*)wo_[1] = pw[1]; SB(); } \
          if (t == 1 && k == 5) { wo_[0] += wst; wo_[1] += wst; wst = 16 * RS; PINV(wo_[0]); PINV(wo_[1]); SB(); } \
          if (t == 1 && k == 6) { fg = *(const volatile LAS u32x4*)fa; SB(); } \
          if (t == 1 && k == 9) VV(mn, min(min3u(fg.x, fg.y, fg.z), fg.w)); \
          if (t == 1 && k == 10) { \
            if (__builtin_expect(__ballot(mn < iv) != 0ull, 0)) { int n_ = 0; do { fg = *(const volatile LAS u32x4*)fa; mn = min(min3u(fg.x, fg.y, fg.z), fg.w); } while (__ballot(mn < iv) != 0ull && ++n_ < (1 << 22)); } \
            asm volatile("" ::: "memory"); SB(); } \
          if (dm) { asm volatile("global_load_lds_dwordx4 %0, %1" :: "v"(dvo[dm ? k - 11 : 0]), "s"(xs) : "memory"); SB(); } \
          if (t == 0 && k >= 13 && k < 17) { xq[(k + 3) & 3] = xr[(k + 3) & 3] + (unsigned)((((T_) + 1) & 3) * 20480); PINV(xq[(k + 3) & 3]); SB(); } \
        } } } while (0)
    unsigned m0k; asm volatile("s_mov_b32 %0, m0" : "=s"(m0k));
    unsigned wst = 0;
#pragma unroll 1
    for (int T = 0; T < 16; T += 2) { A_TILE(T, 0); A_TILE(T + 1, 1); }
    A_TILE(16, 0);
    asm volatile("s_mov_b32 m0, %0" :: "s"(m0k));
    asm volatile("s_nop 7" : "+v"(acc[0][0]), "+v"(acc[0][1]));
#pragma unroll
    for (int t = 0; t < 2; ++t) *(LAS u32x2*)wo_[t] = (u32x2){cvtpk(acc[0][t][0], acc[0][t][1]), cvtpk(acc[0][t][2], acc[0][t][3])};
  }
  VMW(0);
  BAR();
  const int jc = j & 7;
  unsigned ov_[5];
#pragma unroll
  for (int i = 0; i < 5; ++i) { const unsigned c = (unsigned)((wave + 4 * i) * 64 + lane), r = c / 10u; ov_[i] = r * 1280u + (c - r * 10u) * 16u; PINV(ov_[i]); }
#define A_OD(g_) do { _Pragma("unroll") for (int i = 0; i < 5; ++i) glds(a.Awo + 80 * jc + (g_) * (128 * 640), ov_[i], lb + (unsigned)((g_) * 20480 + (wave + 4 * i) * 1024)); } while (0)
  A_OD(0); A_OD(1); A_OD(2); A_OD(3);
  const __amdgpu_buffer_rsrc_t rs_oa = mkrs(ws + O_OA);
#pragma unroll 1
  for (int ps = 0; ps < 2; ++ps) {
    const int rl = 32 * (2 * wave + ps) + (lane & 31), row = 256 * rg + rl, hh = lane >> 5;
    unsigned ra[8];
#pragma unroll
    for (int w = 0; w < 8; ++w) ra[w] = (unsigned)(LR + 48 * hh + (rl + 16 - 2 * w) * RS);
#define A_LD10(d_, ad_) do { const u32x4 x0 = *(const u32x4*)(lds + (ad_)), x1 = *(const u32x4*)(lds + (ad_) + 16); const u32x2 x2 = *(const u32x2*)(lds + (ad_) + 32); \
        d_[0] = x0.x; d_[1] = x0.y; d_[2] = x0.z; d_[3] = x0.w; d_[4] = x1.x; d_[5] = x1.y; d_[6] = x1.z; d_[7] = x1.w; d_[8] = x2.x; d_[9] = x2.y; } while (0)
      unsigned q[10], kv[8][10], vv[8][10];
      A_LD10(q, ra[0]);
#pragma unroll
      for (int w = 0; w < 8; ++w) A_LD10(kv[w], ra[w] + 96);
#pragma unroll
      for (int w = 0; w < 8; ++w) A_LD10(vv[w], ra[w] + 192);
      SB();
      float lg[8], mx = -3.0e38f;
#pragma unroll
      for (int w = 0; w < 8; ++w) lg[w] = 0.f;
#pragma unroll
      for (int i = 0; i < 10; ++i)
#pragma unroll
        for (int w = 0; w < 8; ++w) lg[w] = dot2bf(q[i], kv[w][i], lg[w]);
#pragma unroll
      for (int w = 0; w < 8; ++w) { lg[w] = row - 2 * w >= 0 ? rbf(rbf(lg[w]) * 0.22360679774997896f) : rbf(-1000000000.0f); mx = fmaxf(mx, lg[w]); }
      float sum = 0.f;
#pragma unroll
      for (int w = 0; w < 8; ++w) { lg[w] = __expf(lg[w] - mx); sum += lg[w]; }
      const float inv = __builtin_amdgcn_rcpf(sum);
      float ov[20];
#pragma unroll
      for (int d = 0; d < 20; ++d) ov[d] = 0.f;
#pragma unroll
      for (int w = 0; w < 8; w += 2) {
        const unsigned pw = cvtpk(lg[w] * inv, lg[w + 1] * inv);
#pragma unroll
        for (int i = 0; i < 10; ++i) {
          ov[2 * i] = dot2bf(__builtin_amdgcn_perm(vv[w + 1][i], vv[w][i], 0x05040100u), pw, ov[2 * i]);
          ov[2 * i + 1] = dot2bf(__builtin_amdgcn_perm(vv[w + 1][i], vv[w][i], 0x07060302u), pw, ov[2 * i + 1]);
        }
      }
    unsigned pk[10];
#pragma unroll
    for (int i = 0; i < 10; ++i) pk[i] = cvtpk(ov[2 * i], ov[2 * i + 1]);
    const v2u_t m0 = __builtin_amdgcn_permlane32_swap(pk[0], pk[0], false, false), m1 = __builtin_amdgcn_permlane32_swap(pk[1], pk[1], false, false);
    const unsigned oo = (unsigned)((row >> 4) * 20480 + (row & 15) * 16 + (5 * j + 3 * hh) * 256);
    st16(rs_oa, oo, 0u, hh ? (u32x4){pk[2], pk[3], pk[4], pk[5]} : (u32x4){pk[0], pk[1], pk[2], pk[3]});
    st16(rs_oa, oo + 256u, 0u, hh ? (u32x4){pk[6], pk[7], pk[8], pk[9]} : (u32x4){pk[4], pk[5], pk[6], pk[7]});
    st16(rs_oa, hh ? 0x80000000u : oo + 512u, 0u, (u32x4){pk[8], pk[9], m0[1], m1[1]});
  }
  VMW(0);
  BAR();
  if (wave == 3 && lane == 0) __hip_atomic_fetch_add((gu32*)(cnt + CNT(C_AO + rg)), 1u, RLX_AGENT);
  A_OD(4);
  u32x4 wt[5][20];
  {
    unsigned o_ = (unsigned)((8 * lq + (l15 >> 2)) * 160 + 8 * (l15 & 3)), o2 = o_ + 51200u; PINV(o_); PINV(o2);
#pragma unroll
    for (int kk = 0; kk < 20; ++kk) {
      if (kk == 16) { VMW(0); BAR(); }
#pragma unroll
      for (int t = 0; t < 5; ++t) wt[t][kk] = trrd2(lds, (kk < 10 ? o_ : o2) + (unsigned)((kk % 10) * 5120 + 32 * t), 640u);
      SB();
    }
  }
  wait_ge<4>(cnt + CNT(C_AO + rg), 16u, tmo);
  const int rt0 = 16 * rg + 8 * (j >> 3) + 2 * wave;
  const __amdgpu_buffer_rsrc_t rs_of = mkrs(ws + O_OA + (size_t)rt0 * 20480), rs_out = mkrs(a.out + (size_t)rt0 * (16 * 1280) + 80 * jc);
  unsigned fo = (unsigned)lane * 16u; PINV(fo);
  u32x4 of[16];
#pragma unroll
  for (int n = 0; n < 16; ++n) { of[n] = ld16(rs_of, fo, (unsigned)(n * 1024)); SB(); }
  f32x4 acc[5];
#pragma unroll
  for (int n = 0; n < 40; ++n) {
    const int k = n % 20;
#pragma unroll
    for (int t = 0; t < 5; ++t) {
      if (k == 0) { if (t < 4) asm volatile("v_mfma_f32_16x16x32_bf16 %0, %1, %2, 0" : "=&v"(acc[t]) : "a"(wt[t][k]), "v"(of[n & 15])); else asm volatile("v_mfma_f32_16x16x32_bf16 %0, %1, %2, 0" : "=&v"(acc[t]) : "v"(wt[t][k]), "v"(of[n & 15])); }
      else { if (t < 3 || (t == 3 && k < 3)) asm volatile("v_mfma_f32_16x16x32_bf16 %0, %1, %2, %0" : "+v"(acc[t]) : "a"(wt[t][k]), "v"(of[n & 15])); else asm volatile("v_mfma_f32_16x16x32_bf16 %0, %1, %2, %0" : "+v"(acc[t]) : "v"(wt[t][k]), "v"(of[n & 15])); }
    }
    SB();
    if (n + 16 < 40) { of[n & 15] = ld16(rs_of, fo, (unsigned)((n + 16) * 1024)); SB(); }
    if (k == 19) {
      asm volatile("s_nop 7" : "+v"(acc[0]), "+v"(acc[1]), "+v"(acc[2]), "+v"(acc[3]), "+v"(acc[4]));
      const int ln = lane_id();
#pragma unroll
      for (int t = 0; t < 5; ++t) { const u32x2 w = {cvtpk(acc[t][0], acc[t][1]), cvtpk(acc[t][2], acc[t][3])}; __builtin_amdgcn_raw_buffer_store_b64(w, rs_out, (ln & 15) * 2560 + (ln >> 4) * 8 + 32 * t, (n / 20) * (16 * 2560), 0); }
      SB();
    }
  }
}

__device__ __forceinline__ void calibrate(char* ws, const u16* mem0, const int id, const unsigned tag) {
  unsigned* const cnt = (unsigned*)(ws + O_CNT);
  const int lane = lane_id();
  if (id < 20) {
    const int k = id >> 1;
    const u32x4 v = *(const u32x4*)(mem0 + (lane & 7) * 640 + 64 * k + (lane >> 3) * 8);
    st16(mkrs(ws + O_X1), (unsigned)(lane * 16), (unsigned)((k & 7) * 1024 + (k >> 3) * 16384 + (id & 1) * 8192), (u32x4){min(v.x, 0xfffffffeu), v.z, min(v.y, 0xfffffffeu), v.w});
  }
  {
    const u32x4 ones = {~0u, ~0u, ~0u, ~0u}; const unsigned vo = (unsigned)lane * 16u;
    if (id < 60) st16(mkrs(cnt), vo, (unsigned)id * 1024u, (u32x4){0u, 0u, 0u, 0u});
    { const unsigned ko = 63u * KM_S + ((unsigned)id >> 1) * KM_H + ((unsigned)id & 1u) * 1024u; st16(mkrs(ws + O_KM), vo, ko, ones); st16(mkrs(ws + O_KM), vo, ko + 1024u, ones); }
    const unsigned sl = (unsigned)id >> 3, e = (unsigned)id & 7u;
    static_assert(PRE == 8, "");
#pragma unroll
    for (int i = 0; i < 6; ++i) st16(mkrs(ws + O_PS), vo, sl * 65536u + (e * 6u + (unsigned)i) * 1024u, ones);
#pragma unroll
    for (int i = 0; i < 2; ++i) st16(mkrs(ws + O_X1), vo, (sl + 1u) * X1_STEP + (e * 2u + (unsigned)i) * 1024u, ones);
    if (e < 4u) st16(mkrs(ws + O_X1), vo, (sl + 1u) * X1_STEP + 16384u + (e >> 1) * 8192u + (e & 1u) * 1024u, ones);
    VMW(0);
    if (lane == 0) __hip_atomic_store((gu32*)(cnt + W_DONE + id), tag, RLX_AGENT);
  }
  char* cal = ws + O_CAL + (id & 3) * 16384 + (id >> 2) * 512;
  const unsigned flip = ((unsigned)((size_t)cal >> 21) ^ xcc_side()) & 1u;
  unsigned best[2] = {65535u, 65535u}, acc = 0;
#pragma unroll 1
  for (int r = (id & 4) ? CALN : 0; r < CALN; ++r) {
#pragma unroll
    for (int hf = 0; hf < 2; ++hf) {
      const unsigned long long t0 = __builtin_amdgcn_s_memtime();
      if (lane == 0) acc += __hip_atomic_fetch_add((gu32*)(cal + ((unsigned)hf ^ flip) * 8192u + (acc & 0u)), 1u, RLX_AGENT);
      asm volatile("s_waitcnt vmcnt(0)" ::: "memory");
      const unsigned d = (unsigned)(__builtin_amdgcn_s_memtime() - t0);
      best[hf] = min(best[hf], d);
    }
  }
  init_wait(ws, tag);
  if (lane < 2) __hip_atomic_fetch_max((gu32*)(cnt + CNT(C_FILL) + lane), 65536u - (lane ? best[1] : best[0]) + (acc & 0u), RLX_AGENT);
  VMW(0);
  const unsigned n = (unsigned)__builtin_amdgcn_readfirstlane(__hip_atomic_fetch_add((gu32*)(cnt + CNT(C_FILL) + 2), lane == 0 ? 1u : 0u, RLX_AGENT));
  if (n == 63u) {
    const u32x2 b = ld8(mkrs(cnt + CNT(C_FILL)), 0u, 0u);
    if (lane == 0) __hip_atomic_store((gu32*)(cnt + CNT(C_POL)), b.y > b.x ? 3u : 2u, RLX_AGENT);
  }
}

__global__ __launch_bounds__(256, 1) void k_mega(Args a) {
  extern __shared__ __attribute__((aligned(16))) unsigned char shm[];
  volatile LAS int* sl = (volatile LAS int*)shm;
  unsigned ep = 0;
  if (threadIdx.x < 64u) ep = __hip_atomic_load((const gu32*)(a.ws + O_CNT) + W_EPOCH + (threadIdx.x == 0u ? 0u : 128u + (threadIdx.x & 7u) * 32u), RLX_AGENT);
  else {
    const char* pc; asm volatile("s_getpc_b64 %0" : "=s"(pc)); const unsigned o = ((blockIdx.x >> 3) * 48u + ((threadIdx.x - 64u) >> 2)) * 128u;
    touch(pc + (o < (unsigned)CPF ? o : 0u), lds_addr(shm) + 256u); }
  if (threadIdx.x == 0) {
    const unsigned x0 = (unsigned)__builtin_amdgcn_s_getreg((3 << 11) | 20) & 7u;
    gu32* const cw = (gu32*)(a.ws + O_CNT);
    const unsigned first = x0 * 32u + ((blockIdx.x >> 3) & 31u);
    const unsigned tg = ep + 0x9e3779b1u;
    unsigned slot = first;
#pragma unroll 1
    for (unsigned i = 0; i < 256u; ++i) {
      slot = ((first + (i & ~31u)) & 224u) | ((first + i) & 31u);
      if (__hip_atomic_exchange(cw + W_SLOT + slot, tg, RLX_AGENT) != tg) break;
    }
    if (slot < 128u) {
      constexpr unsigned NS[4] = {LAY_S}, NA[4] = {LAY_A};
      const unsigned x = slot >> 5, k = slot & 31u;
      unsigned s0 = 0, a0 = 0, ns = NS[0], na = NA[0];
#pragma unroll
      for (unsigned i = 1; i < 4u; ++i) if (x >= i) { s0 += NS[i - 1]; a0 += NA[i - 1]; ns = NS[i]; na = NA[i]; }
      const unsigned iS = s0 + k, iA = a0 + k - ns, iR = slot - s0 - a0 - ns - na;
      slot = k < ns ? (iS >> 3) * 32u + (iS & 7u) : (k < ns + na ? (iA >> 2) * 32u + 28u + (iA & 3u) : (iR / 20u) * 32u + 8u + iR % 20u);
    }
    sl[0] = (int)slot; sl[1] = (int)tg;
  }
  __syncthreads();
  const int slot = __builtin_amdgcn_readfirstlane(sl[0]); const unsigned tag = (unsigned)__builtin_amdgcn_readfirstlane(sl[1]);
  VMW(0);
  __syncthreads();
  const int xq = (slot >> 5) & 3, kq = slot & 31, wave = __builtin_amdgcn_readfirstlane(threadIdx.x >> 6);
#define ENTER __builtin_amdgcn_s_waitcnt(0)
  { const int fq = kq - (slot < 128 ? 8 : 12); if (fq >= 0 && fq < 20) f_part(a, shm, (slot >> 7) * 16 + 4 * xq + fq / 5, fq % 5, wave, slot < 128, tag); }
  if (slot < 128) {
    if (kq < 8) {
      const int h = xq * 8 + kq;
      if (wave == 0) { ENTER; s_role<0>(a, shm, h, tag); } else if (wave == 1) { ENTER; s_role<1>(a, shm, h, tag); } else if (wave == 2) { ENTER; s_role<2>(a, shm, h, tag); } else { ENTER; s_role<3>(a, shm, h, tag); }
    } else if (kq < 28) {
      const int u = xq * 20 + kq - 8;
      ENTER; r_role(a, shm, u, wave);
      if (u == 0 && wave == 0 && lane_id() == 0) __hip_atomic_store((gu32*)(a.ws + O_CNT) + W_EPOCH, tag, RLX_AGENT);
    } else {
      ENTER;
      { unsigned d0 = 0, d1 = 0; const unsigned vo = (unsigned)(((kq - 28) * 4 + wave) * 8192 + lane_id() * 128);
        asm volatile("global_load_dword %0, %2, %3\n global_load_dword %1, %4, %5\n s_waitcnt vmcnt(0)" : "+v"(d0), "+v"(d1) : "v"(vo), "s"(a.Bx), "v"(vo < 10240u ? vo : 0u), "s"(a.mem0)); }
      calibrate(a.ws, a.mem0, (xq * 4 + kq - 28) * 4 + wave, tag);
      if (kq & 1) {
        const char* src = (const char*)a.Bx + ((xq * 2 + ((kq - 28) >> 1)) * 4 + wave) * 8192 + lane_id() * 128;
#pragma unroll 1
        for (int i = 0; i < APB; ++i) touch(src + i * 262144, lds_addr(shm));
        VMW(0);
      }
      att_role(a, xq * 8 + (kq - 28) * 2 + (wave & 1), wave >> 1); }
  } else {
    if (kq < 12) { ENTER;
      { unsigned d0 = 0; asm volatile("global_load_dword %0, %1, %2\n s_waitcnt vmcnt(0)" : "+v"(d0) : "v"((unsigned)((kq * 4 + wave) * 8192 + lane_id() * 128)), "s"(a.Bx)); }
      g_role<true>(a, shm, (kq < 6 ? 0 : 18) + xq * 6 + kq, wave, tag); }
    else { ENTER; g_role<false>(a, shm, xq * 20 + kq - 12, wave, tag); }
    { ENTER; int w2 = wave, s2 = slot; asm volatile("" : "+s"(w2), "+s"(s2)); a_role(a, shm, s2 & 15, (s2 >> 4) & 7, w2); }
  }
}

template <int MODE> __global__ __launch_bounds__(256, 1) void k_probe(Args a) {
  const int lane = threadIdx.x & 63, wave = threadIdx.x >> 6;
  const u64 t0 = __builtin_amdgcn_s_memrealtime();
  unsigned acc = 0;
  if (MODE == 0 || MODE == 1) {
    const u16* src[4] = {a.Wq, a.Wk, a.Wv, a.Wo};
#pragma unroll
    for (int r = 0; r < 4; ++r) { const __amdgpu_buffer_rsrc_t rs = mkrs((const char*)src[r] + (size_t)(blockIdx.x * 4 + wave) * 7680);
      u32x4 v[8];
#pragma unroll
      for (int i = 0; i < 8; ++i) v[i] = __builtin_amdgcn_raw_buffer_load_b128(rs, i < 7 || lane < 32 ? lane * 16 : (int)0x80000000u, i * 1024, MODE == 1 ? 2 : 0);
#pragma unroll
      for (int i = 0; i < 8; ++i) acc += v[i].x; }
  } else if (MODE == 2 || MODE == 3 || MODE == 4) {
    const __amdgpu_buffer_rsrc_t rs = mkrs(a.ws + (MODE == 2 ? 4 : (MODE == 3 ? 110 : 220)) * MB + (size_t)(blockIdx.x * 4 + wave) * 30720);
    u32x4 v[30];
#pragma unroll
    for (int i = 0; i < 30; ++i) v[i] = __builtin_amdgcn_raw_buffer_load_b128(rs, lane * 16, i * 1024, 0);
#pragma unroll
    for (int i = 0; i < 30; ++i) acc += v[i].x;
  } else {
    const __amdgpu_buffer_rsrc_t rs = mkrs(a.ws + 4 * MB + (size_t)(blockIdx.x * 4 + wave) * 61440);
#pragma unroll
    for (int i = 0; i < 60; ++i) __builtin_amdgcn_raw_buffer_store_b128((u32x4){1u, 2u, 3u, (unsigned)i}, rs, lane * 16, i * 1024, 16);
  }
  asm volatile("s_waitcnt vmcnt(0)" : "+v"(acc) :: "memory");
  const u64 t1 = __builtin_amdgcn_s_memrealtime();
  const u64 te = t1 + 10 * (t1 - t0) + (acc == 0x12345u ? 1 : 0);
  while (__builtin_amdgcn_s_memrealtime() < te) __builtin_amdgcn_s_sleep(8);
}

extern "C" void kernel_launch(void* const* d_in, const int* in_sizes, int n_in, void* d_out, int out_size, void* d_ws, size_t ws_size, hipStream_t stream) {
  Args a;
  a.Ax = (const u16*)d_in[0]; a.Awqkv = (const u16*)d_in[1]; a.Awo = (const u16*)d_in[2]; a.Bx = (const u16*)d_in[3]; a.mem0 = (const u16*)d_in[4];
  a.Wq = (const u16*)d_in[5]; a.Wk = (const u16*)d_in[6]; a.Wv = (const u16*)d_in[7]; a.Wo = (const u16*)d_in[8];
  a.out = (u16*)d_out;
  a.ws = (char*)(((size_t)d_ws + 2 * MB - 1) & ~(2 * MB - 1));
  if ((size_t)(a.ws - (char*)d_ws) + O_END > ws_size) return;
  const int dd[8] = {DDV};
  for (int i = 0; i < 8; ++i) a.d[i] = dd[i];
  static_assert(CNT(C_TOTAL) * 4 == 15 * 4096, "");
  (void)hipFuncSetAttribute((const void*)k_mega, hipFuncAttributeMaxDynamicSharedMemorySize, 163840);
  k_probe<4><<<256, 256, 0, stream>>>(a);
  k_mega<<<256, 256, 163840, stream>>>(a);
}
```

```cpp
#include <hip/hip_runtime.h>
#include <hip/hip_bf16.h>
#include <stdint.h>

typedef unsigned short u16;
typedef unsigned long long u64;
typedef __attribute__((ext_vector_type(4))) float f32x4;
typedef __attribute__((address_space(1))) u64 gu64;
typedef __attribute__((address_space(1))) unsigned gu32;
typedef unsigned u32x8 __attribute__((ext_vector_type(8)));
typedef unsigned u32x4 __attribute__((ext_vector_type(4)));
typedef unsigned u32x2 __attribute__((ext_vector_type(2)));
typedef __attribute__((ext_vector_type(4))) short bf16x4;
typedef __attribute__((ext_vector_type(8))) __bf16 bfv8;
typedef __attribute__((ext_vector_type(16))) __bf16 bfv16;
typedef __bf16 bf16x2_t __attribute__((ext_vector_type(2)));
typedef float f32x2_t __attribute__((ext_vector_type(2)));
typedef unsigned v2u_t __attribute__((ext_vector_type(2)));
#define RLX_AGENT __ATOMIC_RELAXED, __HIP_MEMORY_SCOPE_AGENT
#define LAS __attribute__((address_space(3)))

__device__ __forceinline__ float bf2f(u16 v) { return __uint_as_float(((unsigned)v) << 16); }
__device__ __forceinline__ u16 f2bf(float f) { return __builtin_bit_cast(u16, (__bf16)f); }
__device__ __forceinline__ float rbf(float f) { return bf2f(f2bf(f)); }
__device__ __forceinline__ unsigned cvtpk(float lo, float hi) { f32x2_t v = {lo, hi}; return __builtin_bit_cast(unsigned, __builtin_convertvector(v, bf16x2_t)); }
__device__ __forceinline__ __amdgpu_buffer_rsrc_t mkrs(const void* p) { return __builtin_amdgcn_make_buffer_rsrc((void*)p, 0, 0x7fffffff, 0x00020000); }
__device__ __forceinline__ u32x4 ld16(__amdgpu_buffer_rsrc_t rs, unsigned voff, unsigned soff) { return __builtin_amdgcn_raw_buffer_load_b128(rs, (int)voff, (int)soff, 16); }
__device__ __forceinline__ u32x2 ld8(__amdgpu_buffer_rsrc_t rs, unsigned voff, unsigned soff) { return __builtin_amdgcn_raw_buffer_load_b64(rs, (int)voff, (int)soff, 16); }
__device__ __forceinline__ void st16(__amdgpu_buffer_rsrc_t rs, unsigned voff, unsigned soff, u32x4 w) { __builtin_amdgcn_raw_buffer_store_b128(w, rs, (int)voff, (int)soff, 16); }
__device__ __forceinline__ void st8(__amdgpu_buffer_rsrc_t rs, unsigned voff, unsigned soff, unsigned lo, unsigned hi) { u32x2 w = {lo, hi}; __builtin_amdgcn_raw_buffer_store_b64(w, rs, (int)voff, (int)soff, 16); }
__device__ __forceinline__ float max16(float x) { v2u_t r = __builtin_amdgcn_permlane16_swap(__float_as_uint(x), __float_as_uint(x), false, false); return fmaxf(__uint_as_float(r[0]), __uint_as_float(r[1])); }
__device__ __forceinline__ float max32(float x) { v2u_t r = __builtin_amdgcn_permlane32_swap(__float_as_uint(x), __float_as_uint(x), false, false); return fmaxf(__uint_as_float(r[0]), __uint_as_float(r[1])); }
__device__ __forceinline__ float sum16(float x) { v2u_t r = __builtin_amdgcn_permlane16_swap(__float_as_uint(x), __float_as_uint(x), false, false); return __uint_as_float(r[0]) + __uint_as_float(r[1]); }
__device__ __forceinline__ float sum32(float x) { v2u_t r = __builtin_amdgcn_permlane32_swap(__float_as_uint(x), __float_as_uint(x), false, false); return __uint_as_float(r[0]) + __uint_as_float(r[1]); }
__device__ __forceinline__ float tred32(float a, float b) { v2u_t r = __builtin_amdgcn_permlane32_swap(__float_as_uint(a), __float_as_uint(b), false, false); return __uint_as_float(r[0]) + __uint_as_float(r[1]); }
__device__ __forceinline__ unsigned half_of(const char* base, unsigned off, unsigned side, unsigned pol) {
  return off + ((((unsigned)(((size_t)base + off) >> 21) ^ side ^ pol) & 1u) << 13);
}
__device__ __forceinline__ unsigned side_map(const void* base, unsigned L, unsigned side, unsigned pol) { return half_of((const char*)base, (L >> 13) << 14, side, pol) + (L & 8191u); }
constexpr unsigned CH_SPIN_CAP = 1u << 18;
__device__ __forceinline__ bool spin_check(unsigned& spins, unsigned* tmop) {
  asm volatile("" ::: "memory");
  ++spins;
  if ((spins & 63u) == 0u) {
    gu32* tmo = (gu32*)tmop;
    if (__builtin_amdgcn_readfirstlane(__hip_atomic_load(tmo, RLX_AGENT))) return false;
    if (spins > CH_SPIN_CAP) { __hip_atomic_store(tmo, 1u, RLX_AGENT); return false; }
  }
  return true;
}
__device__ __forceinline__ unsigned xcc_side() { return ((unsigned)__builtin_amdgcn_s_getreg((3 << 11) | 20) >> 2) & 1u; }
__device__ __forceinline__ f32x4 mfma(u32x4 a, u32x4 b, f32x4 c) { return __builtin_amdgcn_mfma_f32_16x16x32_bf16(__builtin_bit_cast(bfv8, a), __builtin_bit_cast(bfv8, b), c, 0, 0, 0); }
__device__ __forceinline__ f32x4 smf(u32x4 a, u32x8 b, f32x4 c, int ix) { return __builtin_amdgcn_smfmac_f32_16x16x64_bf16(__builtin_bit_cast(bfv8, a), __builtin_bit_cast(bfv16, b), c, ix, 0, 0); }
__device__ __forceinline__ u32x4 lo4(u32x8 v) { return __builtin_shufflevector(v, v, 0, 1, 2, 3); }
__device__ __forceinline__ u32x4 hi4(u32x8 v) { return __builtin_shufflevector(v, v, 4, 5, 6, 7); }
__device__ __forceinline__ u32x8 cat8(u32x4 a, u32x4 b) { return __builtin_shufflevector(a, b, 0, 1, 2, 3, 4, 5, 6, 7); }
#define BAR() do { asm volatile("s_waitcnt lgkmcnt(0)" ::: "memory"); __builtin_amdgcn_s_barrier(); asm volatile("" ::: "memory"); } while (0)
#define SB() __builtin_amdgcn_sched_barrier(0)
#define VMW(n) asm volatile("s_waitcnt vmcnt(" #n ")" ::: "memory")
#define ZERO4 ((f32x4){0.f, 0.f, 0.f, 0.f})
__device__ __forceinline__ u32x2 trrd(const unsigned char* lds, unsigned off) { return __builtin_bit_cast(u32x2, __builtin_amdgcn_ds_read_tr16_b64_v4i16((LAS bf16x4*)(lds + off))); }
__device__ __forceinline__ u32x4 trrd2(const unsigned char* lds, unsigned off, unsigned d) { const u32x2 lo = trrd(lds, off), hi = trrd(lds, off + d); return (u32x4){lo.x, lo.y, hi.x, hi.y}; }

#define TSK 8
#define APN 2
#define APU 1
#define GFW 6
#define APB 10
#define OPH 3
#define RSELF 3
#define RN3 20
#define XSL 0
#define NU1 4
#define AQ0 2
#define AU0 3
#define LAY_S 16, 0, 16, 0
#define LAY_A 8, 0, 8, 0
#define DUR0 132u
#define BKO 2
#define RFW 4
#define CPF 120800
#define FD0 6
#define CALN 5
#define RLAG 4
#define RN1 7
#define RN2 9
#define DDV 68, 74, 8, 8, 0, 2, 1, 88
#define FSL 4
struct Args {
  const u16* mem0; const u16* Wq; const u16* Wk; const u16* Wv; const u16* Wo; const u16* Bx; const u16* Ax; const u16* Awqkv; const u16* Awo;
  u16* out; char* ws;
  int d[8];
};
constexpr size_t MB = 1u << 20;
constexpr size_t O_CNT = 1 * MB, O_CAL = O_CNT + 65536, O_X1 = 2 * MB;
constexpr unsigned X1_STEP = 32768;
constexpr size_t O_FF = 4 * MB, O_KF = O_FF + 25 * MB, O_QF = O_KF + 24 * MB, O_KM = O_QF + 24 * MB, O_PT = O_KM + 7 * MB, O_UO = O_PT + 8 * MB,
                 O_PS = O_UO + 96 * MB, O_OA = O_PS + 8 * MB, O_END = O_OA + 3 * MB;
static_assert(O_X1 + 64 * 32768 <= O_FF, "");
#define PT_BLK(g_, c_) ((((g_) * ((g_) + 3) / 2 + ((c_) == 4 ? 0 : (c_) + 1)) >> 1) * 16384 + (((g_) * ((g_) + 3) / 2 + ((c_) == 4 ? 0 : (c_) + 1)) & 1) * 4096)
constexpr unsigned PT_SEG = 131072;
constexpr unsigned QK_H = 64 * 12288, QK_S = 12288, U_U = 64 * 16384, U_S = 16384, KM_H = 64 * 3072, KM_S = 3072;
#define CNT(i) (32 * (i))
constexpr int PRE = 8;
#define PSLOT(s_) (((s_) >> 5) * (4 << 20) + ((s_) & 31) * 65536)
constexpr int C_TMO = 8, C_POL = 9, C_FILL = 10, C_AO = 16, C_PT_ = 32, C_PS = 64, C_PO = 96, C_TOK = 192, C_FF = 448, C_TOTAL = 480;
__device__ __forceinline__ float nopk(float x) { asm("" : "+v"(x)); return x; }
__device__ __forceinline__ float fmx(float a, float b) { float big = __builtin_inff(); asm("" : "+s"(big)); return __builtin_amdgcn_fmed3f(a, b, big); }
__device__ __forceinline__ float t32(float a, float b) { return nopk(tred32(a, b)); }
__device__ __forceinline__ int lane_id() { int l = (int)__builtin_amdgcn_mbcnt_hi(~0u, __builtin_amdgcn_mbcnt_lo(~0u, 0u)); asm volatile("" : "+v"(l)); return l; }
#define RT_MARK(t_) asm volatile("s_memrealtime %0\n s_waitcnt lgkmcnt(0)" : "=s"(t_) :: "memory")
__device__ __forceinline__ unsigned wait_rt(unsigned target) {
  int r;
#define RT_S "s_memrealtime vcc\n s_waitcnt lgkmcnt(0)\n s_sub_i32 %0, %1, vcc_lo\n"
#define RT_F RT_S "s_cmp_le_i32 %0, 4\n s_cbranch_scc1 2f\n"
  asm volatile("0: " RT_S "s_cmp_gt_i32 %0, 400\n s_cbranch_scc1 3f\n s_cmp_le_i32 %0, 13\n s_cbranch_scc1 1f\n s_sleep 1\n s_branch 0b\n"
               "1: " RT_F RT_F RT_F RT_F RT_F "s_branch 1b\n"
               "2: s_cmp_lt_i32 %0, 2\n s_cbranch_scc1 3f\n s_nop 2\n s_cmp_lt_i32 %0, 3\n s_cbranch_scc1 3f\n s_nop 2\n s_cmp_lt_i32 %0, 4\n s_cbranch_scc1 3f\n s_nop 6\n 3:"
               : "=&s"(r) : "s"(__builtin_amdgcn_readfirstlane(target)) : "vcc", "scc", "memory");
  return (unsigned)r - 1u < 4u ? target : target - (unsigned)r;
}
__device__ __forceinline__ unsigned ts_max(unsigned v) {
  if (__ballot(v == ~0u)) return ~0u;
  const unsigned base = (unsigned)__builtin_amdgcn_readfirstlane(v) - 4096u;
  unsigned d = v - base;
  d = max(d, (unsigned)__builtin_amdgcn_mov_dpp((int)d, 0x121, 0xf, 0xf, true)); d = max(d, (unsigned)__builtin_amdgcn_mov_dpp((int)d, 0x122, 0xf, 0xf, true));
  d = max(d, (unsigned)__builtin_amdgcn_mov_dpp((int)d, 0x124, 0xf, 0xf, true)); d = max(d, (unsigned)__builtin_amdgcn_mov_dpp((int)d, 0x128, 0xf, 0xf, true));
  return (base + max((unsigned)__builtin_amdgcn_readlane(d, 0), (unsigned)__builtin_amdgcn_readlane(d, 16)));
}
constexpr int TS_OFF = 32768 + 4096;
__device__ __forceinline__ unsigned ts_wait(__amdgpu_buffer_rsrc_t rs, int so) {
  unsigned v = ~0u;
  for (int n = 0; n < 64 && v == ~0u; ++n) { __builtin_amdgcn_s_sleep(2); asm volatile("" ::: "memory"); v = ts_max(__builtin_amdgcn_raw_buffer_load_b32(rs, (lane_id() & 31) * 128, so, 16)); }
  return v;
}
__device__ __forceinline__ int adapt(int d, unsigned failed, int base, int s, int up, int mask) {
  int r;
  asm volatile("s_add_i32 %0, %1, -1\n s_max_i32 %0, %0, %3\n s_and_b32 vcc_lo, %4, %6\n s_cselect_b32 %0, %1, %0\n s_add_i32 vcc_lo, %1, %5\n s_cmp_lg_u32 %2, 0\n s_cselect_b32 %0, vcc_lo, %0\n s_cmp_gt_i32 %4, 3\n s_cselect_b32 %0, %0, %1"
               : "=&s"(r) : "s"(__builtin_amdgcn_readfirstlane(d)), "s"(__builtin_amdgcn_readfirstlane(failed)), "s"(base), "s"(s), "s"(up), "s"(mask) : "vcc", "scc");
  return r;
}
#define ADAPT(d_, failed_, base_) d_ = adapt(d_, failed_, base_, s, a.d[5], a.d[6])
#define PINV(x) asm volatile("" : "+v"(x))
constexpr int L_MEM = 0, L_QI = 10240;
constexpr int L_B0 = 16384, L_B1 = 65536, L_B2 = 114688;
constexpr int L_KX = L_B1, L_FLG = 14080, L_ST = 14336, L_KI = L_B1 + 28672;

#define AFR(k_) (*(const u32x4*)(lds + afb + (k_) * 1024))

#define TR8_ASM "ds_read_b64_tr_b16 %0, %8\n ds_read_b64_tr_b16 %1, %8 offset:1536\n ds_read_b64_tr_b16 %2, %8 offset:12288\n ds_read_b64_tr_b16 %3, %8 offset:13824\n" \
               "ds_read_b64_tr_b16 %4, %8 offset:24576\n ds_read_b64_tr_b16 %5, %8 offset:26112\n ds_read_b64_tr_b16 %6, %8 offset:36864\n ds_read_b64_tr_b16 %7, %8 offset:38400\n s_waitcnt lgkmcnt(0)"
__device__ __forceinline__ void tr8(const unsigned ad, u32x8& f0, u32x8& f1) {
  u32x2 a, b, c, d, e, f, g, h;
  asm volatile(TR8_ASM : "=&v"(a), "=&v"(b), "=&v"(c), "=&v"(d), "=&v"(e), "=&v"(f), "=&v"(g), "=&v"(h) : "v"(ad) : "memory");
  f0 = (u32x8){a.x, a.y, b.x, b.y, c.x, c.y, d.x, d.y}; f1 = (u32x8){e.x, e.y, f.x, f.y, g.x, g.y, h.x, h.y};
}
__device__ __forceinline__ void glds(const void* sbase, unsigned voff, unsigned ldsdst) {
  unsigned keep;
  asm volatile("s_mov_b32 %0, m0\n s_mov_b32 m0, %3\n s_nop 0\n global_load_lds_dwordx4 %1, %2\n s_mov_b32 m0, %0" : "=&s"(keep) : "v"(voff), "s"(sbase), "s"(__builtin_amdgcn_readfirstlane(ldsdst)) : "memory");
}
__device__ __forceinline__ void touch(const void* p, unsigned ldsdump) {
  unsigned keep;
  asm volatile("s_mov_b32 %0, m0\n s_mov_b32 m0, %2\n s_nop 0\n global_load_lds_dword %1, off\n s_mov_b32 m0, %0" : "=&s"(keep) : "v"(p), "s"(__builtin_amdgcn_readfirstlane(ldsdump)) : "memory");
}
__device__ __forceinline__ unsigned lds_addr(const unsigned char* p) { return (unsigned)(size_t)(const LAS unsigned char*)p; }
__device__ __forceinline__ unsigned ld_word(const unsigned* p) { return (unsigned)__builtin_amdgcn_readfirstlane(__hip_atomic_load((const gu32*)p, RLX_AGENT)); }
template <int SLEEP>
__device__ __forceinline__ unsigned wait_ge(const unsigned* p, unsigned target, unsigned* tmo) {
  unsigned spins = 0, v;
  while ((v = ld_word(p)) < target) { __builtin_amdgcn_s_sleep(SLEEP); if (!spin_check(spins, tmo)) break; }
  asm volatile("" ::: "memory");
  return v;
}
constexpr int W_DONE = CNT(C_TOTAL), W_EPOCH = W_DONE + 128, W_SLOT = W_DONE + 256;
__device__ __forceinline__ void init_wait(const char* ws, unsigned tag) {
  const gu32* p = (const gu32*)(ws + O_CNT) + W_DONE + lane_id();
  for (unsigned n = 0; n < CH_SPIN_CAP && !__all(__hip_atomic_load(p, RLX_AGENT) == tag); ++n) __builtin_amdgcn_s_sleep(8);
  asm volatile("" ::: "memory");
}
template <int SLEEP>
__device__ __forceinline__ unsigned wait_t(const unsigned* p, const bool both, unsigned target, unsigned* tmo) {
  unsigned spins = 0, v;
  for (;;) {
    const u32x4 w = ld16(mkrs(p), 0u, 0u);
    v = min(min(w.x, w.y), w.z);
    if (both) { const u32x4 w2 = ld16(mkrs(p), 16u, 0u); v = min(v, min(min(w2.x, w2.y), w2.z)); }
    v = (unsigned)__builtin_amdgcn_readfirstlane(v);
    if (v >= target) break;
    __builtin_amdgcn_s_sleep(SLEEP); if (!spin_check(spins, tmo)) break;
  }
  asm volatile("" ::: "memory");
  return v;
}
__device__ __forceinline__ unsigned wait_u(const unsigned* p, unsigned target, unsigned* tmo) {
  unsigned spins = 0, v;
  for (;;) {
    const u32x4 w = ld16(mkrs(p), 0u, 0u);
    v = (unsigned)__builtin_amdgcn_readfirstlane(min(min(w.x, w.y), min(w.z, w.w)));
    if (v >= target) break;
    __builtin_amdgcn_s_sleep(4); if (!spin_check(spins, tmo)) break;
  }
  asm volatile("" ::: "memory");
  return v;
}
__device__ __forceinline__ unsigned min3u(unsigned a, unsigned b, unsigned c) { return min(min(a, b), c); }
template <int W>
__device__ __forceinline__ void s_role(const Args& a, unsigned char* lds, const int h, const unsigned tag) {
  char* ws = a.ws; asm volatile("" : "+s"(ws));
  unsigned* const cnt = (unsigned*)(ws + O_CNT); unsigned* const tmo = cnt + CNT(C_TMO);
  char* const X1 = ws + O_X1; char* const P = ws + O_PS;
  const int lane = lane_id(), l15 = lane & 15, lq = lane >> 4;
  constexpr int wave = W;
#define WG00 "{a[0:7]}"
#define WG01 "{a[8:15]}"
#define WG02 "{a[16:23]}"
#define WG03 "{a[24:31]}"
#define WG04 "{a[32:39]}"
#define WG05 "{a[40:47]}"
#define WG06 "{a[48:55]}"
#define WG07 "{a[56:63]}"
#define WG10 "{a[64:71]}"
#define WG11 "{a[72:79]}"
#define WG12 "{a[80:87]}"
#define WG13 "{a[88:95]}"
#define WG14 "{a[96:103]}"
#define WG15 "{a[104:111]}"
#define WG16 "{a[112:119]}"
#define WG17 "{a[120:127]}"
#define WG20 "{a[128:135]}"
#define WG21 "{a[136:143]}"
#define WG22 "{a[144:151]}"
#define WG23 "{a[152:159]}"
#define WG24 "{a[160:167]}"
#define WG25 "{a[168:175]}"
#define WG26 "{a[176:183]}"
#define WG27 "{a[184:191]}"
#define WG30 "{a[192:199]}"
#define WG31 "{a[200:207]}"
#define WG32 "{a[208:215]}"
#define WG33 "{a[216:223]}"
#define WG34 "{a[224:231]}"
#define WG35 "{a[232:239]}"
#define WG36 "{v[88:95]}"
#define WG37 "{v[96:103]}"
#define WG40 "{v[104:111]}"
#define WG41 "{v[112:119]}"
#define WG42 "{v[120:127]}"
#define WG43 "{v[128:135]}"
#define WG44 "{v[136:143]}"
#define WG45 "{v[144:151]}"
#define WG46 "{v[152:159]}"
#define WG47 "{v[160:167]}"
#define WG50 "{v[168:175]}"
#define WG51 "{v[176:183]}"
#define WG52 "{v[184:191]}"
#define WG53 "{v[192:199]}"
#define WG54 "{v[200:207]}"
#define WG55 "{v[208:215]}"
#define WG56 "{v[216:223]}"
#define WG57 "{v[224:231]}"
#define TRR(c, a, b, o) "ds_read_b64_tr_b16 " #c "[" #a ":" #b "], %2 offset:" #o "\n"
#define TW00 TRR(a, 0, 1, 0) TRR(a, 2, 3, 1536) TRR(a, 4, 5, 12288) TRR(a, 6, 7, 13824) TRR(a, 8, 9, 24576) TRR(a, 10, 11, 26112) TRR(a, 12, 13, 36864) TRR(a, 14, 15, 38400)
#define TW01 TRR(a, 16, 17, 0) TRR(a, 18, 19, 1536) TRR(a, 20, 21, 12288) TRR(a, 22, 23, 13824) TRR(a, 24, 25, 24576) TRR(a, 26, 27, 26112) TRR(a, 28, 29, 36864) TRR(a, 30, 31, 38400)
#define TW02 TRR(a, 32, 33, 0) TRR(a, 34, 35, 1536) TRR(a, 36, 37, 12288) TRR(a, 38, 39, 13824) TRR(a, 40, 41, 24576) TRR(a, 42, 43, 26112) TRR(a, 44, 45, 36864) TRR(a, 46, 47, 38400)
#define TW03 TRR(a, 48, 49, 0) TRR(a, 50, 51, 1536) TRR(a, 52, 53, 12288) TRR(a, 54, 55, 13824) TRR(a, 56, 57, 24576) TRR(a, 58, 59, 26112) TRR(a, 60, 61, 36864) TRR(a, 62, 63, 38400)
#define TW10 TRR(a, 64, 65, 0) TRR(a, 66, 67, 1536) TRR(a, 68, 69, 12288) TRR(a, 70, 71, 13824) TRR(a, 72, 73, 24576) TRR(a, 74, 75, 26112) TRR(a, 76, 77, 36864) TRR(a, 78, 79, 38400)
#define TW11 TRR(a, 80, 81, 0) TRR(a, 82, 83, 1536) TRR(a, 84, 85, 12288) TRR(a, 86, 87, 13824) TRR(a, 88, 89, 24576) TRR(a, 90, 91, 26112) TRR(a, 92, 93, 36864) TRR(a, 94, 95, 38400)
#define TW12 TRR(a, 96, 97, 0) TRR(a, 98, 99, 1536) TRR(a, 100, 101, 12288) TRR(a, 102, 103, 13824) TRR(a, 104, 105, 24576) TRR(a, 106, 107, 26112) TRR(a, 108, 109, 36864) TRR(a, 110, 111, 38400)
#define TW13 TRR(a, 112, 113, 0) TRR(a, 114, 115, 1536) TRR(a, 116, 117, 12288) TRR(a, 118, 119, 13824) TRR(a, 120, 121, 24576) TRR(a, 122, 123, 26112) TRR(a, 124, 125, 36864) TRR(a, 126, 127, 38400)
#define TW20 TRR(a, 128, 129, 0) TRR(a, 130, 131, 1536) TRR(a, 132, 133, 12288) TRR(a, 134, 135, 13824) TRR(a, 136, 137, 24576) TRR(a, 138, 139, 26112) TRR(a, 140, 141, 36864) TRR(a, 142, 143, 38400)
#define TW21 TRR(a, 144, 145, 0) TRR(a, 146, 147, 1536) TRR(a, 148, 149, 12288) TRR(a, 150, 151, 13824) TRR(a, 152, 153, 24576) TRR(a, 154, 155, 26112) TRR(a, 156, 157, 36864) TRR(a, 158, 159, 38400)
#define TW22 TRR(a, 160, 161, 0) TRR(a, 162, 163, 1536) TRR(a, 164, 165, 12288) TRR(a, 166, 167, 13824) TRR(a, 168, 169, 24576) TRR(a, 170, 171, 26112) TRR(a, 172, 173, 36864) TRR(a, 174, 175, 38400)
#define TW23 TRR(a, 176, 177, 0) TRR(a, 178, 179, 1536) TRR(a, 180, 181, 12288) TRR(a, 182, 183, 13824) TRR(a, 184, 185, 24576) TRR(a, 186, 187, 26112) TRR(a, 188, 189, 36864) TRR(a, 190, 191, 38400)
#define TW30 TRR(a, 192, 193, 0) TRR(a, 194, 195, 1536) TRR(a, 196, 197, 12288) TRR(a, 198, 199, 13824) TRR(a, 200, 201, 24576) TRR(a, 202, 203, 26112) TRR(a, 204, 205, 36864) TRR(a, 206, 207, 38400)
#define TW31 TRR(a, 208, 209, 0) TRR(a, 210, 211, 1536) TRR(a, 212, 213, 12288) TRR(a, 214, 215, 13824) TRR(a, 216, 217, 24576) TRR(a, 218, 219, 26112) TRR(a, 220, 221, 36864) TRR(a, 222, 223, 38400)
#define TW32 TRR(a, 224, 225, 0) TRR(a, 226, 227, 1536) TRR(a, 228, 229, 12288) TRR(a, 230, 231, 13824) TRR(a, 232, 233, 24576) TRR(a, 234, 235, 26112) TRR(a, 236, 237, 36864) TRR(a, 238, 239, 38400)
#define TW33 TRR(v, 88, 89, 0) TRR(v, 90, 91, 1536) TRR(v, 92, 93, 12288) TRR(v, 94, 95, 13824) TRR(v, 96, 97, 24576) TRR(v, 98, 99, 26112) TRR(v, 100, 101, 36864) TRR(v, 102, 103, 38400)
#define TW40 TRR(v, 104, 105, 0) TRR(v, 106, 107, 1536) TRR(v, 108, 109, 12288) TRR(v, 110, 111, 13824) TRR(v, 112, 113, 24576) TRR(v, 114, 115, 26112) TRR(v, 116, 117, 36864) TRR(v, 118, 119, 38400)
#define TW41 TRR(v, 120, 121, 0) TRR(v, 122, 123, 1536) TRR(v, 124, 125, 12288) TRR(v, 126, 127, 13824) TRR(v, 128, 129, 24576) TRR(v, 130, 131, 26112) TRR(v, 132, 133, 36864) TRR(v, 134, 135, 38400)
#define TW42 TRR(v, 136, 137, 0) TRR(v, 138, 139, 1536) TRR(v, 140, 141, 12288) TRR(v, 142, 143, 13824) TRR(v, 144, 145, 24576) TRR(v, 146, 147, 26112) TRR(v, 148, 149, 36864) TRR(v, 150, 151, 38400)
#define TW43 TRR(v, 152, 153, 0) TRR(v, 154, 155, 1536) TRR(v, 156, 157, 12288) TRR(v, 158, 159, 13824) TRR(v, 160, 161, 24576) TRR(v, 162, 163, 26112) TRR(v, 164, 165, 36864) TRR(v, 166, 167, 38400)
#define TW50 TRR(v, 168, 169, 0) TRR(v, 170, 171, 1536) TRR(v, 172, 173, 12288) TRR(v, 174, 175, 13824) TRR(v, 176, 177, 24576) TRR(v, 178, 179, 26112) TRR(v, 180, 181, 36864) TRR(v, 182, 183, 38400)
#define TW51 TRR(v, 184, 185, 0) TRR(v, 186, 187, 1536) TRR(v, 188, 189, 12288) TRR(v, 190, 191, 13824) TRR(v, 192, 193, 24576) TRR(v, 194, 195, 26112) TRR(v, 196, 197, 36864) TRR(v, 198, 199, 38400)
#define TW52 TRR(v, 200, 201, 0) TRR(v, 202, 203, 1536) TRR(v, 204, 205, 12288) TRR(v, 206, 207, 13824) TRR(v, 208, 209, 24576) TRR(v, 210, 211, 26112) TRR(v, 212, 213, 36864) TRR(v, 214, 215, 38400)
#define TW53 TRR(v, 216, 217, 0) TRR(v, 218, 219, 1536) TRR(v, 220, 221, 12288) TRR(v, 222, 223, 13824) TRR(v, 224, 225, 24576) TRR(v, 226, 227, 26112) TRR(v, 228, 229, 36864) TRR(v, 230, 231, 38400)
#define TWL(n, j, k0, k1, ad_) asm volatile(TW##n##j "s_waitcnt lgkmcnt(0)" : "=&" WG##n##k0(wt[n][k0]), "=&" WG##n##k1(wt[n][k1]) : "v"(ad_) : "memory")
#define TWN(n, j_, ad_) do { if ((j_) == 0) TWL(n, 0, 0, 1, ad_); else if ((j_) == 1) TWL(n, 1, 2, 3, ad_); else if ((j_) == 2) TWL(n, 2, 4, 5, ad_); else TWL(n, 3, 6, 7, ad_); } while (0)
  u32x8 wt[6][8];
  unsigned tro[3];
  {
    const unsigned row = (unsigned)(8 * lq + (l15 >> 2)), swz = ((row >> 1) & 1u) * 2u + ((row >> 3) & 1u) * 4u;
#pragma unroll
    for (int t = 0; t < 3; ++t) tro[t] = row * 384u + (((unsigned)(2 * (4 * t + wave) + ((l15 & 3) >> 1))) ^ swz) * 16u + (unsigned)(l15 & 1) * 8u;
  }
#define SBUF(i_) (((i_) % 3) == 0 ? L_B0 : (((i_) % 3) == 1 ? L_B1 : L_B2))
  unsigned svo[12]; const int sq0 = 3 * ((h >> 2) & 3);
#pragma unroll
  for (int q_ = 0; q_ < 12; ++q_) { const int qr_ = q_ + sq0, ins = wave + 4 * (qr_ < 12 ? qr_ : qr_ - 12); const unsigned c = (unsigned)(ins * 64 + lane), r = c / 24u, pc = c - r * 24u, sw = ((r >> 1) & 1u) * 2u + ((r >> 3) & 1u) * 4u;
    svo[q_] = ((r & ~10u) | ((r & 2u) << 2) | ((r & 8u) >> 2)) * 12288u + 16u * (pc ^ sw); PINV(svo[q_]); }
#define SDMA(i_) do { const char* W_ = (const char*)((((i_) & 1) ? a.Wk : a.Wq) + (size_t)(128 * (((i_) >> 1) < 4 ? ((((i_) >> 1) + h) & 3) : 4)) * 6144 + 192 * h);        \
    _Pragma("unroll") for (int q_ = 0; q_ < 12; ++q_) { const int qr_ = q_ + sq0; glds(W_, svo[q_], lds_addr(lds) + (unsigned)(SBUF(i_) + (wave + 4 * (qr_ < 12 ? qr_ : qr_ - 12)) * 1024)); } SB(); } while (0)
  SDMA(0); SDMA(1); SDMA(2);
#pragma unroll
  for (int i = 0; i < 10; ++i) {
    if (i < 8) VMW(24); else if (i == 8) VMW(12); else { init_wait(ws, tag); VMW(0); }
    BAR();
    u32x8 res[3][2];
    const unsigned tb = lds_addr(lds) + (unsigned)SBUF(i); const int sl = ((i >> 1) - W) & 3;
    if (i >= 8) {
#pragma unroll
      for (int t = 0; t < 3; ++t) tr8(tb + tro[t], res[t][0], res[t][1]);
    } else if (i & 1) { TWN(3, sl, tb + tro[0]); TWN(4, sl, tb + tro[1]); TWN(5, sl, tb + tro[2]); }
    else { TWN(0, sl, tb + tro[0]); TWN(1, sl, tb + tro[1]); TWN(2, sl, tb + tro[2]); }
    BAR();
    if (i + 3 < 10) SDMA(i + 3);
    if (i >= 8) {
#pragma unroll
      for (int t = 0; t < 3; ++t)
#pragma unroll
        for (int k = 0; k < 2; ++k) {
          unsigned char* dst = lds + SBUF(i) + wave * 12288 + t * 4096 + k * 2048 + lane * 16;
          *(u32x4*)dst = lo4(res[t][k]); *(u32x4*)(dst + 1024) = hi4(res[t][k]);
        }
    }
  }
  const unsigned pol = wait_ge<8>(cnt + CNT(C_POL), 1u, tmo) & 1u;
  const __amdgpu_buffer_rsrc_t rs_p = mkrs(P), rs_kf = mkrs(ws + O_KF + (size_t)h * QK_H), rs_tp = mkrs(cnt + CNT(C_PT_ + h));
  unsigned tprog = 0;
  constexpr int kxs = 6 * W;
  if (W < 2) {
    tprog = wait_t<4>(cnt + CNT(C_PT_ + h), false, 1u, tmo);
#pragma unroll
    for (int i = 0; i < 6; ++i) *(u32x4*)(lds + L_KX + (kxs + i) * 1024 + lane * 16) = ld16(rs_kf, (unsigned)((kxs + i) * 1024 + lane * 16), 0u);
  } else if (W == 2) { if (lane < 32) *(unsigned*)(lds + L_FLG + 4 * lane) = lane == 24 ? DUR0 + 8u : 0u; *(u32x4*)(lds + L_ST + lane * 16) = (u32x4){0u, 0u, 0u, 0u}; }
  const unsigned lb = lds_addr(lds);
  unsigned afb = lb + (unsigned)(lane * 16), trq = (unsigned)(L_B2 + wave * 12288) + afb; PINV(trq);
#define trk (afb + (unsigned)(L_B0 + wave * 12288))
#define LP(T_, ad_) ((LAS T_*)(ad_))
  const __amdgpu_buffer_rsrc_t rs_x1 = mkrs(X1 - lb), rs_kn = mkrs(ws + O_KF + (size_t)h * QK_H + kxs * 1024 - lb);
  int ix = l15 < 8 ? 0x4444 : 0xEEEE; PINV(ix); int ix2 = (l15 & 2) ? 0xEEEE : 0x4444;
  const unsigned cp = (unsigned)(2 * (lq & 1) + (lq >> 1));
  unsigned qwa = lb + (unsigned)(L_QI + wave * 320 + ((l15 >> 2) * 2 + (l15 & 1)) * 32 + ((l15 >> 1) & 1) * 4) + cp * 8u, kwa = lb + (unsigned)(L_KI + (16 * wave + l15) * 16) + cp * 4u;
  unsigned qra = lb + (unsigned)(L_QI + (2 * (lq >> 1) + (l15 >> 3)) * 320 + (lq & 1) * 128 + ((l15 >> 2) & 1) * 32 + (l15 & 3) * 8);
  unsigned kra = lb + (unsigned)(L_KI + (4 * lq + (l15 >> 2)) * 16 + (l15 & 1) * 8);
  unsigned lwa = lb + (unsigned)(L_ST + 2 * lq * 128 + (16 * wave + l15) * 4), fl0 = lb + (unsigned)L_FLG;
  PINV(afb); PINV(qwa); PINV(kwa); PINV(fl0); if (W < 3) { PINV(qra); PINV(ix2); } if (W >= 2) PINV(kra); if (W < 2) PINV(lwa);
  unsigned kxb = (unsigned)(L_KX + wave * 6144) + afb; if (W < 2) PINV(kxb);
  const bool hi8 = l15 >= 8;
  const __amdgpu_buffer_rsrc_t rs_ts = mkrs(P + half_of(P, 0u, 0u, pol) + TS_OFF);
  const __amdgpu_buffer_rsrc_t rs_ps = mkrs(cnt + CNT(C_PS));
  int tso = lane == 0 ? 128 * h : (int)0x80000000u; if (W == 3) PINV(tso);
  unsigned po = side_map(P, (unsigned)((4 * h + ((lane & 7) >> 1)) * 128 + (lane >> 3) * 16 + (lane & 1) * 8), 0u, pol);
  unsigned pm = (lane & 1) == 0 ? side_map(P, (unsigned)(16384 + h * 128 + lane * 2), 0u, pol) : 0x80000000u;
  float lsc = 1.4426950408889634f * 0.07216878364870323f;
  if (W == 2) { PINV(po); PINV(pm); asm volatile("" : "+s"(lsc)); }
  const unsigned xh = half_of(X1, 0u, 0u, pol), gb = (unsigned)((W + h) & 3) * 2048u;
  unsigned lg0 = 0, tb2 = 0, dur = DUR0, tsv = ~0u; int dly = a.d[0] + 2 * a.d[4]; asm volatile("" : "+s"(dly));
  BAR();
  constexpr int P0 = 2 * W, P1 = 2 * W + 1, R2 = W >> 1;
#define OT(i_) ((2 * W + 2 + (i_)) & 7)
#define AR0 "v[232:235]"
#define AR1 "v[236:239]"
#define AR2 "v[240:243]"
#define AR3 "v[244:247]"
#define AR4 "v[248:251]"
#define AR5 "v[252:255]"
#define SW01 "v_permlane32_swap_b32 v232, v234\n"
#define SW02 "v_permlane32_swap_b32 v233, v235\n"
#define SW11 "v_permlane32_swap_b32 v236, v238\n"
#define SW12 "v_permlane32_swap_b32 v237, v239\n"
#define SW21 "v_permlane32_swap_b32 v240, v242\n"
#define SW22 "v_permlane32_swap_b32 v241, v243\n"
#define SW31 "v_permlane32_swap_b32 v244, v246\n"
#define SW32 "v_permlane32_swap_b32 v245, v247\n"
#define SW41 "v_permlane32_swap_b32 v248, v250\n"
#define SW42 "v_permlane32_swap_b32 v249, v251\n"
#define SMI "v_smfmac_f32_16x16x64_bf16 %0, %1, %2, %3"
#define SMC(M_, c_, ...) do { if ((c_) == 0) M_("v", __VA_ARGS__); else if ((c_) == 2) M_("{a[240:247]}", __VA_ARGS__); else M_("{a[248:255]}", __VA_ARGS__); SB(); } while (0)
#define SM_(bc, n, f_, w_, x_) asm volatile(SMI : "+{" AR##n "}"(ac[n]) : "v"(f_), bc(w_), "v"(x_))
#define SMP_(bc, n, f_, w_, x_, m, p) asm volatile(SW##m##p "v_smfmac_f32_16x16x64_bf16 %0, %2, %3, %4" : "+{" AR##n "}"(ac[n]), "+{" AR##m "}"(ac[m]) : "v"(f_), bc(w_), "v"(x_))
#define RC(j_) ((j_) == 1 ? 2 : ((j_) == 3 ? 3 : 0))
#define MW(n, f_, k) do { SM_(WG##n##k, n, f_, wt[n][k], ix); SB(); } while (0)
#define MO(n, k) MW(n, A[k - 2], k)
#define MR(n, f_, j_) SMC(SM_, RC(j_), n, f_, r##j_, ix)
#define MOP(n, k, m, p) do { SMP_(WG##n##k, n, A[k - 2], wt[n][k], ix, m, p); SB(); } while (0)
#define MRP(n, f_, j_, m, p) SMC(SMP_, RC(j_), n, f_, r##j_, ix, m, p)
#define SL(i_, j_) SMC(SM_, RC(j_), 0, qf[i_], r##j_, ix2)
#define SLP(i_, j_, m, p) SMC(SMP_, RC(j_), 0, qf[i_], r##j_, ix2, m, p)
#define APIN(n) asm volatile("" : "+{" AR##n "}"(ac[n]))
#define ZL() do { asm volatile("v_mov_b64 v[232:233], 0\n v_mov_b64 v[234:235], 0" : "={v[232:235]}"(ac[0])); SB(); } while (0)
#define RLO(tb_, t_, k2_) (*LP(const u32x4, (tb_) + (t_) * 4096 + (k2_) * 2048))
#define RHI(tb_, t_, k2_) (*LP(const u32x4, (tb_) + (t_) * 4096 + (k2_) * 2048 + 1024))
#define SFR(k_) (*LP(const u32x4, afb + (k_) * 1024))
#define TR1(ad_) __builtin_bit_cast(u32x2, __builtin_amdgcn_ds_read_tr16_b64_v4i16(LP(bf16x4, ad_)))
#define TR2(ad_, d_) __builtin_shufflevector(TR1(ad_), TR1((ad_) + (d_)), 0, 1, 2, 3)
#define RFR(tb_, t_, k2_) cat8(RLO(tb_, t_, k2_), RHI(tb_, t_, k2_))
#define DS(x) do { x; SB(); } while (0)
#define VV(v_, x) do { v_ = (x); PINV(v_); SB(); } while (0)
#define FLG(st_) do { asm volatile("" ::: "memory"); *LP(volatile unsigned, fl0 + (st_) * 16 + W * 4) = snv; SB(); } while (0)
#define FLGV(st_, v_) do { asm volatile("" ::: "memory"); *LP(volatile unsigned, fl0 + (st_) * 16 + W * 4) = v_; SB(); } while (0)
#define MEET() do { asm volatile("s_barrier" ::: "memory"); SB(); } while (0)
#define FRD(g_, st_) do { asm volatile("" ::: "memory"); g_ = *LP(const volatile u32x4, fl0 + (st_) * 16); asm volatile("" ::: "memory"); SB(); } while (0)
#define FCK(g_) do { mn = min3u(mn, g_.x, g_.y); mn = min3u(mn, g_.z, g_.w); PINV(mn); SB(); } while (0)
#define AD0(n_) VV(e0, ac[n_][0] + ac[n_][2])
#define AD1(n_) VV(e1, ac[n_][1] + ac[n_][3])
#define CVT(w_) VV(w_, cvtpk(e0, e1))
#define FOLD5() do { asm volatile("s_nop 7\n v_permlane32_swap_b32 v252, v254\n v_permlane32_swap_b32 v253, v255\n v_add_f32 v252, v252, v254\n v_add_f32 v253, v253, v255\n v_cvt_pk_bf16_f32 v252, v252, v253" : "+{" AR5 "}"(ac[5])); SB(); } while (0)
#define WR(ad_, w_) DS(*LP(unsigned, ad_) = w_)
#define QFR(j_) qf[j_] = TR2(qra + (j_) * 1280, 64u)
#define KMR(j_) cat8(TR2(kra + (j_) * 1024, 256u), TR2(kra + (j_) * 1024 + 512, 256u))
#define KXR(j_) RFR(kxb, 0, j_)
#pragma unroll 1
  for (int s = 0; s < 64; ++s) {
    u32x8 r0 = RFR(trq, 0, R2), r1 = RFR(trq, 1, R2), r2 = RFR(trq, 2, R2), r3 = RFR(trq, 0, 1 - R2);
    int pss = PSLOT(s); asm volatile("" : "+s"(pss));
    f32x4 ac[6] = {ZERO4, ZERO4, ZERO4, ZERO4, ZERO4, ZERO4};
    unsigned snv = (unsigned)(s + 1);
    unsigned so = xh + (unsigned)s * X1_STEP, so2 = so + 16384u + (unsigned)R2 * 1024u; so += gb; unsigned so1 = so + 1024u;
    asm volatile("" : "+v"(snv), "+s"(so), "+s"(so1), "+s"(so2));
    APIN(0); APIN(1); APIN(2); APIN(3); APIN(4); APIN(5);
    lg0 = wait_rt(tsv + (unsigned)dly);
    u32x4 g0, g1, g2; unsigned m, spins = 0;
#define G_OWN(H_) do { g0 = ld16(rs_x1, afb, so); SB(); g1 = ld16(rs_x1, afb, so1); SB(); g2 = ld16(rs_x1, afb, so2); SB(); H_; SB(); \
      MW(0, g0, 0); DS(*LP(u32x4, afb + P0 * 1024) = g0); \
      MW(1, g0, 0); VV(m, max(g0.x, g0.z)); \
      MW(2, g0, 0); DS(*LP(u32x4, afb + P1 * 1024) = g1); \
      MW(0, g1, 1); VV(m, max(max(m, g1.x), g1.z)); \
      MW(1, g1, 1); if ((W & 1) == 0) DS(*LP(u32x4, afb + (8 + R2) * 1024) = g2); VV(m, max(max(m, g2.x), g2.z)); \
      MW(2, g1, 1); } while (0)
    G_OWN(if (W == 3) { __builtin_amdgcn_raw_buffer_store_b32(min(lg0 + dur, 0xfffffffeu), rs_ts, tso, pss, 16); VMW(4);        \
      __builtin_amdgcn_raw_buffer_store_b32((unsigned)s, rs_ps, tso, 0, 16); });
    if (__builtin_expect(__ballot(m == ~0u) != 0ull, 0)) {

      const int to = lane_id() == 0 ? 128 * h : (int)0x80000000u; unsigned tn;
      for (;;) {
        __builtin_amdgcn_raw_buffer_store_b32(~0u, rs_ts, to, pss, 16);
        if (spins > 2u) __builtin_amdgcn_s_sleep(BKO);
        ac[0] = ZERO4; ac[1] = ZERO4; ac[2] = ZERO4; APIN(0); APIN(1); APIN(2);
        tn = (unsigned)__builtin_amdgcn_s_memrealtime() + *LP(const volatile unsigned, fl0 + 96);
        G_OWN((void)0);
        const bool go = spin_check(spins, tmo); if (__ballot(m == ~0u) == 0ull || !go) break; }
      __builtin_amdgcn_raw_buffer_store_b32(min(tn, 0xfffffffeu), rs_ts, to, pss, 16);
    }
    MR(0, g2, 0);
    BAR();
    u32x4 A[7], lo, hi; float e0, e1; unsigned w0, w1, w2; u32x8 r4;
    DS(A[0] = SFR(OT(0)));
    MR(1, g2, 1); DS(A[1] = SFR(OT(1)));
    MR(2, g2, 2); DS(A[2] = SFR(OT(2)));
    MW(3, g0, 0); DS(A[3] = SFR(OT(3)));
    MW(4, g0, 0); DS(A[4] = SFR(OT(4)));
    MW(5, g0, 0); DS(A[5] = SFR(OT(5)));
    MW(3, g1, 1); DS(A[6] = SFR(9 - R2));
    MW(4, g1, 1); DS(lo = RLO(trq, 1, 1 - R2));
    MW(5, g1, 1); DS(hi = RHI(trq, 1, 1 - R2)); r0 = cat8(lo, hi);
    MO(0, 2); DS(lo = RLO(trq, 2, 1 - R2));
    MO(0, 3); DS(hi = RHI(trq, 2, 1 - R2)); r1 = cat8(lo, hi);
    MO(0, 4); DS(lo = RLO(trk, 0, R2));
    MO(0, 5); DS(hi = RHI(trk, 0, R2)); r2 = cat8(lo, hi);
    MO(0, 6); DS(lo = RLO(trk, 0, 1 - R2));
    MO(0, 7); DS(hi = RHI(trk, 0, 1 - R2)); r4 = cat8(lo, hi);
    MR(0, A[6], 3);
    MR(1, A[6], 0); DS(lo = RLO(trk, 1, R2));
    MO(1, 2);
    MOP(1, 3, 0, 1);
    MOP(1, 4, 0, 2); AD0(0);
    MO(1, 5); AD1(0);
    MO(1, 6); CVT(w0);
    MO(1, 7); DS(hi = RHI(trk, 1, R2)); r3 = cat8(lo, hi);
    MR(2, A[6], 1); DS(lo = RLO(trk, 1, 1 - R2));
    MO(2, 2);
    MOP(2, 3, 1, 1);
    MOP(2, 4, 1, 2); AD0(1);
    MO(2, 5); AD1(1);
    MO(2, 6); CVT(w1);
    MO(2, 7); DS(hi = RHI(trk, 1, 1 - R2)); r0 = cat8(lo, hi);
    MR(3, g2, 2); if (W >= 2) DS(lo = RLO(trk, 2, R2));
    MR(3, A[6], 4);
    MRP(4, g2, 3, 2, 1);
    MOP(3, 2, 2, 2); AD0(2); AD1(2);
    MO(3, 3); WR(qwa, w0); WR(qwa + 1280, w1); CVT(w2); WR(qwa + 2560, w2); FLG(0);
    u32x4 qf[3], g; unsigned mn = snv;
    if (W < 2) {
      u32x4 l2;
      MO(3, 4); DS(l2 = RLO(kxb, 0, 0));
      MO(3, 5); DS(hi = RHI(kxb, 0, 0)); r4 = cat8(l2, hi);
      MO(3, 6); DS(l2 = RLO(kxb, 0, 1));
      MO(3, 7); DS(hi = RHI(kxb, 0, 1)); r3 = cat8(l2, hi);
      MR(4, A[6], 0); FRD(g, 0); DS(QFR(0));
      MO(4, 2); DS(QFR(1));
      MOP(4, 3, 3, 1); DS(QFR(2));
      MOP(4, 4, 3, 2); DS(l2 = RLO(kxb, 0, 2)); AD0(3);
      MO(4, 5); DS(hi = RHI(kxb, 0, 2)); r0 = cat8(l2, hi); AD1(3);
      MO(4, 6); VV(mn, min3u(mn, g.x, g.y)); ZL();
      MO(4, 7); VV(mn, min3u(mn, g.z, g.w));
      SL(0, 4); CVT(w0);
      SL(1, 3); DS(lo = RLO(trk, 2, R2));
      SLP(2, 0, 4, 1); DS(hi = RHI(trk, 2, R2)); r1 = cat8(lo, hi);
      MOP(5, 2, 4, 2); DS(lo = RLO(trk, 2, 1 - R2)); AD0(4);
      MO(5, 3); DS(hi = RHI(trk, 2, 1 - R2)); r2 = cat8(lo, hi); AD1(4);
      MO(5, 4); CVT(w1); AD0(0); AD1(0);
      MO(5, 5); DS(*LP(float, lwa) = e0; *LP(float, lwa + 128) = e1); FLGV(4, mn); WR(kwa, w0); WR(kwa + 1024, w1); FLG(2);
      MO(5, 6); MO(5, 7); MEET();
      MR(5, g2, 1); MR(5, A[6], 2);
      FOLD5(); WR(kwa + 2048, __float_as_uint(ac[5][0])); FLG(3); MEET();
      if (__builtin_expect(__ballot(mn != snv) != 0ull, 0)) {
        int n_ = 0; do { mn = snv; FRD(g, 0); FCK(g); } while (__ballot(mn != snv) != 0ull && ++n_ < 65536);
        QFR(0); QFR(1); QFR(2); r4 = KXR(0); r3 = KXR(1); r0 = KXR(2); ZL(); asm volatile("s_nop 1"); SL(0, 4); SL(1, 3); SL(2, 0);
        asm volatile("s_nop 7" : "+{" AR0 "}"(ac[0]));
        AD0(0); AD1(0);
        *LP(float, lwa) = e0; *LP(float, lwa + 128) = e1; FLG(4);
      }
    } else {
      MO(3, 4); DS(hi = RHI(trk, 2, R2)); r1 = cat8(lo, hi);
      MO(3, 5); DS(lo = RLO(trk, 2, 1 - R2));
      MO(3, 6); DS(hi = RHI(trk, 2, 1 - R2)); r2 = cat8(lo, hi);
      MO(3, 7);
      MR(4, A[6], 0); if (W == 2) FRD(g, 0);
      MO(4, 2);
      MOP(4, 3, 3, 1);
      MOP(4, 4, 3, 2); if (W == 2) DS(QFR(0)); AD0(3);
      MO(4, 5); if (W == 2) DS(QFR(1)); AD1(3);
      MO(4, 6); if (W == 2) DS(QFR(2)); CVT(w0);
      MO(4, 7); if (W == 2) VV(mn, min3u(mn, g.x, g.y));
      MR(5, g2, 1); if (W == 2) VV(mn, min3u(mn, g.z, g.w));
      MR(5, A[6], 2);
      MOP(5, 2, 4, 1);
      MOP(5, 3, 4, 2); AD0(4);
      MO(5, 4); AD1(4);
      MO(5, 5); WR(kwa, w0); CVT(w1); WR(kwa + 1024, w1); FLG(2);
      MO(5, 6); if (W == 2) ZL();
      MO(5, 7);
      FOLD5(); WR(kwa + 2048, __float_as_uint(ac[5][0])); FLG(3);
      MEET();
      if (W == 3) MEET();
      if (W == 2) {
      u32x4 g3; u32x2 g4; f32x4 xv;
#define XRD() do { asm volatile("" ::: "memory"); g4 = *LP(const volatile u32x2, fl0 + 64); asm volatile("" ::: "memory"); xv = *LP(const volatile f32x4, afb + L_ST); SB(); } while (0)
      XRD();
      FRD(g, 2); DS(r0 = KMR(0)); DS(r1 = KMR(1));
      MEET();
      FRD(g3, 3); DS(r2 = KMR(2));
      float ml, x0, x1, x2, x3;
      constexpr float LS = 1.4426950408889634f * 0.07216878364870323f;
#define TOKM() VV(ml, fmx(fmaxf(fmaxf(xv[0], xv[1]), xv[2]), xv[3]))
      TOKM();
      VV(x0, xv[0] - ml);
      SL(0, 0); VV(x1, xv[1] - ml);
      SL(1, 1); VV(x2, xv[2] - ml); VV(x3, xv[3] - ml); VV(x0, x0 * LS); VV(x1, x1 * LS);
      VV(mn, min3u(mn, g4.x, g4.y)); FCK(g);
      SL(2, 2);
      FCK(g3);
      VV(x0, __builtin_amdgcn_exp2f(x0)); VV(x1, __builtin_amdgcn_exp2f(x1));
      if (__builtin_expect(__ballot(mn != snv) != 0ull, 0)) {
        int n_ = 0;
        do { FRD(g3, 3); XRD(); mn = min3u(snv, g4.x, g4.y); FCK(g3); } while (__ballot(mn != snv) != 0ull && ++n_ < 65536);
        QFR(0); QFR(1); QFR(2);
        ZL(); r0 = KMR(0); r1 = KMR(1); r2 = KMR(2); SB(); SL(0, 0); SL(1, 1); SL(2, 2);
        TOKM();
        x0 = __builtin_amdgcn_exp2f(nopk(xv[0] - ml) * LS); x1 = __builtin_amdgcn_exp2f(nopk(xv[1] - ml) * LS); x2 = xv[2] - ml; x3 = xv[3] - ml;
        asm volatile("s_nop 7" : "+v"(x0), "+v"(x1), "+v"(x2), "+v"(x3));
      }
      u32x2 pt; unsigned pmw;
#define DP(c_) " " c_ " row_mask:0xf bank_mask:0xf\n"
      asm volatile("v_mul_f32 %5, %8, %5\n v_mul_f32 %6, %8, %6\n"
                   "v_add_f32_dpp v240, v232, v234 quad_perm:[0,1,2,3] row_mask:0xf bank_mask:0x3\n v_add_f32_dpp v240, v233, v235 quad_perm:[0,1,2,3] row_mask:0xf bank_mask:0xc\n"
                   "v_max_f32 v241, v240, %7\n v_exp_f32 %5, %5\n v_exp_f32 %6, %6\n"
                   "v_max_f32_dpp v241, v241, v241" DP("quad_perm:[1,0,3,2]") "v_add_f32 v242, %3, %4\n v_add_f32 v243, %5, %6\n"
                   "v_max_f32_dpp v241, v241, v241" DP("quad_perm:[2,3,0,1]") "v_add_f32 v242, v242, v243\n s_nop 0\n"
                   "v_max_f32_dpp v241, v241, v241" DP("row_half_mirror")
                   "v_sub_f32 v240, v240, v241\n v_sub_f32 v241, %7, v241\n v_mul_f32 v240, %8, v240\n v_mul_f32 v241, %8, v241\n v_exp_f32 v240, v240\n v_exp_f32 v241, v241\n s_nop 0\n"
                   "v_fma_f32 v242, v242, v241, v240\n v_mul_f32 %3, %3, v241\n v_mul_f32 %4, %4, v241\n"
                   "v_add_f32_dpp v242, v242, v242" DP("quad_perm:[1,0,3,2]") "v_mul_f32 %5, %5, v241\n v_mul_f32 %6, %6, v241\n"
                   "v_add_f32_dpp v242, v242, v242" DP("quad_perm:[2,3,0,1]") "v_mov_b32_dpp v243, v240" DP("quad_perm:[1,0,3,2]") "s_nop 0\n"
                   "v_add_f32_dpp v242, v242, v242" DP("row_half_mirror")
                   "v_rcp_f32 v242, v242\n s_nop 0\n"
                   "v_mul_f32 %3, %3, v242\n v_mul_f32 %4, %4, v242\n v_mul_f32 %5, %5, v242\n v_mul_f32 %6, %6, v242\n"
                   "v_cvt_pk_bf16_f32 v236, |%3|, |%4|\n v_cvt_pk_bf16_f32 v237, |%5|, |%6|\n"
                   "buffer_store_dwordx2 v[236:237], %9, %11, %12 offen sc1\n"
                   "v_mul_f32 v240, v240, v242\n v_mul_f32 v243, v243, v242\n v_cvt_pk_bf16_f32 %1, |v240|, |v243|\n"
                   "buffer_store_dword %1, %10, %11, %12 offen sc1"
                   : "=&{v[236:237]}"(pt), "=&v"(pmw), "+{" AR0 "}"(ac[0]), "+v"(x0), "+v"(x1), "+v"(x2), "+v"(x3)
                   : "v"(ml), "s"(lsc), "v"(po), "v"(pm), "s"(rs_p), "s"(pss) : "v240", "v241", "v242", "v243", "memory");
      SB();
      u64 t2; RT_MARK(t2);
      *LP(volatile u32x2, fl0 + 80) = (u32x2){(unsigned)t2, snv}; SB();
      }
    }
#define LWAIT(v_, off_) do { int n_ = 0; for (;;) { v_ = *LP(const volatile u32x2, fl0 + (off_)); if ((unsigned)__builtin_amdgcn_readfirstlane(v_.y) == (unsigned)(s + 1) || ++n_ >= (1 << 20)) break; __builtin_amdgcn_s_sleep(1); } asm volatile("" ::: "memory"); } while (0)
    if (W < 2) {
      u32x4 v[6], tpl; unsigned tsl;
      const int sn = s < 63 ? s + 1 : 63;
      if (W == 0) { tsl = __builtin_amdgcn_raw_buffer_load_b32(rs_ts, (lane_id() & 31) * 128, pss, 16); SB(); }
      if (__builtin_expect(tprog < (unsigned)(sn + 1), 0)) tprog = wait_t<4>(cnt + CNT(C_PT_ + h), false, (unsigned)(sn + 1), tmo);
#pragma unroll
      for (int i = 0; i < 6; ++i) v[i] = ld16(rs_kn, afb, (unsigned)sn * QK_S + (unsigned)(i * 1024));
      tpl = ld16(rs_tp, 0u, 0u);
      SB();
      if (W == 0) {
        tsv = ts_max(tsl);
        if (__builtin_expect(tsv == ~0u, 0) && s < 63) tsv = ts_wait(rs_ts, pss);
        *LP(volatile u32x2, fl0 + 104) = (u32x2){tsv, snv}; SB();
      }
#pragma unroll
      for (int i = 0; i < 6; ++i) *LP(u32x4, kxb + i * 1024) = v[i];
      tprog = (unsigned)__builtin_amdgcn_readfirstlane(min3u(tpl.x, tpl.y, tpl.z));
    }
    if (W == 3) {
      const int ln = lane_id();
      unsigned ko = (unsigned)(((ln >> 3) & 1) * 512 + (ln >> 4) * 128 + (ln & 7) * 16);
      unsigned eo = ln < 32 ? side_map(P, (unsigned)(h * 512 + ln * 16), 0u, pol) : (ln < 48 ? side_map(P, (unsigned)(16384 + (ln & 8) * 512 + h * 128 + (ln & 7) * 16), 0u, pol) : 0x80000000u);
      PINV(ko); PINV(eo); SB();
      u32x4 kv[3];
      { int n_ = 0; do { mn = snv; FRD(g, 3); FCK(g); } while (__builtin_expect(__ballot(mn != snv) != 0ull, 0) && ++n_ < 65536); }
#pragma unroll
      for (int kk = 0; kk < 3; ++kk) {
        const u32x4 v1 = TR2(kra + kk * 1024, 256u), v2 = TR2(kra + kk * 1024 + 512, 256u);
        kv[kk] = (u32x4){min(hi8 ? v2.x : v1.x, 0xfffffffeu), hi8 ? v2.y : v1.y, min(hi8 ? v2.z : v1.z, 0xfffffffeu), hi8 ? v2.w : v1.w};
      }
      asm volatile("" : "+v"(kv[0]), "+v"(kv[1]), "+v"(kv[2])); SB();
      u32x2 tt = {0u, 0u};
      if (s < 63) LWAIT(tt, 80);
      if (XSL) __builtin_amdgcn_s_sleep(XSL);
      const __amdgpu_buffer_rsrc_t rs_km = mkrs(ws + O_KM);
#pragma unroll
      for (int kk = 0; kk < 3; ++kk) st16(rs_km, ko + (unsigned)(kk * 1024), (unsigned)s * KM_S + (unsigned)h * KM_H, kv[kk]);
      if (s + PRE < 63) st16(rs_p, eo, (unsigned)PSLOT(s + PRE), (u32x4){~0u, ~0u, ~0u, ~0u});
      SB();
      tb2 = (unsigned)__builtin_amdgcn_readfirstlane(tt.x);
      dur = min(dur + 1u, tb2 - lg0); *LP(volatile unsigned, fl0 + 96) = dur + (unsigned)a.d[3];
    }
    ADAPT(dly, spins, a.d[0]);
    if (W != 0) { u32x2 tt; LWAIT(tt, 104); tsv = (unsigned)__builtin_amdgcn_readfirstlane(tt.x); }
  }
#undef trk
}

constexpr int NSELF = RSELF;
__device__ __forceinline__ void f_part(const Args& a, unsigned char* lds, const int h, const int part, const int wave, const bool isr, const unsigned tag) {
  const int lane = lane_id(), l15 = lane & 15, lq = lane >> 4;
  const unsigned lb = lds_addr(lds);
  const int kk = 4 * part + wave;
  u32x4 af[2][6];
  {
    const unsigned vo = (unsigned)((8 * (l15 >> 2) + (l15 & 3)) * 12288 + lq * 16);
    const __amdgpu_buffer_rsrc_t rv = mkrs(a.Wv + 192 * h + (size_t)(32 * kk) * 6144);
#pragma unroll
    for (int t = 0; t < 2; ++t)
#pragma unroll
      for (int e = 0; e < 6; ++e) af[t][e] = __builtin_amdgcn_raw_buffer_load_b128(rv, (int)(vo + 64u * e), 4 * t * 12288, 0);
  }
  SB();
  unsigned dv; { const unsigned r = (unsigned)(8 * wave + (lane >> 3)); dv = r * 1280u + (((unsigned)lane & 7u) ^ (((r >> 1) & 1u) * 2u + ((r >> 3) & 1u) * 4u)) * 16u; PINV(dv); }
  const int g0 = 2 * part;
#define F_COL(g_) ((g_) + g0 < 10 ? (g_) + g0 : (g_) + g0 - 10)
#define F_DMA(g_, b_) do { const u16* s_ = a.Wo + (size_t)(192 * h) * 640 + 64 * F_COL(g_); _Pragma("unroll") for (int q = 0; q < 6; ++q) glds(s_ + q * (32 * 640), dv, lb + (b_) + (unsigned)((wave + 4 * q) * 1024)); } while (0)
#define F_RA(g_) (16384u + (unsigned)((g_) < 4 ? 3 - (g_) : ((g_) < 10 ? 9 - (g_) : 0)) * 24576u)
#pragma unroll 1
  for (int g = 0; g < FD0; ++g) F_DMA(g, F_RA(g));
  unsigned xv[5];
#pragma unroll
  for (int i = 0; i < 5; ++i) { const unsigned c = (unsigned)((wave + 4 * i) * 64 + lane), r = c / 80u; xv[i] = (c ^ r) * 16u + (r >> 2) * 5120u; PINV(xv[i]); }
#define F_XD(sg_, hf_, ad_) do { _Pragma("unroll") for (int i = 0; i < 5; ++i) glds(a.Bx + (size_t)(32 * (sg_) + 4 * (hf_)) * 640, xv[i], lb + (ad_) + (unsigned)(wave * 1024 + i * 4096)); } while (0)
  const __amdgpu_buffer_rsrc_t rs = mkrs(a.ws + O_FF + (size_t)((h >> 1) * 20 + kk) * 1024);
  unsigned vo = 0x80000000u, vo1 = (unsigned)((l15 >> 3) * (16 * 20 * 1024) + (h & 1) * 512 + lq * 128 + (l15 & 7) * 16);
  unsigned bo[4], bq[4];
  { const unsigned brow = (unsigned)(8 * lq + (l15 >> 2)), bg = ((brow >> 1) & 1u) * 2u + ((brow >> 3) & 1u) * 4u;
#pragma unroll
    for (int j = 0; j < 4; ++j) { bo[j] = lb + brow * 128u + (((unsigned)(2 * j + ((l15 & 3) >> 1))) ^ bg) * 16u + (unsigned)(l15 & 1) * 8u; PINV(bo[j]); bq[j] = bo[j] + F_RA(0); } }
#define F_TR(j_, n_) __builtin_bit_cast(u32x2, __builtin_amdgcn_ds_read_tr16_b64_v4i16((LAS bf16x4*)(bq[j_] + ((n_) >> 1) * 4096 + ((n_) & 1) * 512)))
  u32x2 bf[2][12]; f32x4 acc[2][2] = {{ZERO4, ZERO4}, {ZERO4, ZERO4}};
  if (FD0 == 2) VMW(6); else if (FD0 == 3) VMW(12); else if (FD0 == 4) VMW(18); else VMW(30);
  BAR();
#pragma unroll
  for (int n = 0; n < 12; ++n) bf[0][n] = F_TR(0, n);
  unsigned so = 0;
#pragma unroll 1
  for (int g = 0; g < 10; ++g) {
#pragma unroll
    for (int j = 0; j < 4; ++j) {
      unsigned w[4];
      if (g == 0 && j < 6 - FD0) F_DMA(FD0 + j, F_RA(FD0 + j));
      if (j == 3) { VMW(24); asm volatile("s_barrier" ::: "memory");
        if (g < 4) F_DMA(g + 6, F_RA(g));
        else if (g < 8 || (isr && NSELF > 2)) F_XD((isr ? 0 : NSELF) + ((g - 4) >> 1), ~g & 1, 143360u - (unsigned)(g - 4) * 20480u);
#pragma unroll
        for (int q = 0; q < 4; ++q) { bq[q] = bo[q] + F_RA(g + 1); PINV(bq[q]); }
        SB(); }
#pragma unroll
      for (int n = 0; n < 12; ++n) {
        const int e = n >> 1, t = n & 1;
        const u32x4 b = {bf[j & 1][2 * e].x, bf[j & 1][2 * e].y, bf[j & 1][2 * e + 1].x, bf[j & 1][2 * e + 1].y};
        if (e == 0) asm volatile("v_mfma_f32_16x16x32_bf16 %0, %1, %2, 0" : "=&v"(acc[j & 1][t]) : "v"(af[t][e]), "v"(b));
        else asm volatile("v_mfma_f32_16x16x32_bf16 %0, %1, %2, %0" : "+v"(acc[j & 1][t]) : "v"(af[t][e]), "v"(b));
        SB();
        bf[(j + 1) & 1][n] = F_TR((j + 1) & 3, n); SB();
        if (n >= 3 && n < 7) { const f32x4 c = acc[(j + 1) & 1][(n - 3) >> 1]; w[n - 3] = ((n - 3) & 1) ? cvtpk(c[2], c[3]) : cvtpk(c[0], c[1]); PINV(w[n - 3]); SB(); }
        if (n == 8) { st16(rs, vo, so, (u32x4){w[0], w[1], w[2], w[3]}); vo = vo1; so = (unsigned)(4 * F_COL(g) + j) * (2u * 16u * 20u * 1024u); SB(); }
      }
    }
  }
  asm volatile("s_nop 7" : "+v"(acc[1][0]), "+v"(acc[1][1]));
  st16(rs, vo, so, (u32x4){cvtpk(acc[1][0][0], acc[1][0][1]), cvtpk(acc[1][0][2], acc[1][0][3]), cvtpk(acc[1][1][0], acc[1][1][1]), cvtpk(acc[1][1][2], acc[1][1][3])});
  init_wait(a.ws, tag);
  VMW(0);
  BAR();
  if (wave == 0 && lane < 8) __hip_atomic_fetch_add((gu32*)((unsigned*)(a.ws + O_CNT) + CNT(C_FF + (h >> 3) * 8 + lane)), 1u, RLX_AGENT);
}
#define F_WAIT(rep_) do { wait_ge<FSL>(cnt + CNT(C_FF + 8 * wave + (rep_)), 40u, tmo); } while (0)
#define F_LD(t_, k_) ld16(rs_f, (unsigned)((l15 >> 3) * 512 + lq * 128 + (l15 & 7) * 16), (unsigned)(((t_) * 20 + (k_)) * 1024))
#define FOLD(o_, a_, b_) do { \
    const float f0_ = __int_as_float(__builtin_amdgcn_update_dpp(__float_as_int(o_[0]), __float_as_int(o_[0]), 0x128, 0xc, 0xf, false)), f1_ = __int_as_float(__builtin_amdgcn_update_dpp(__float_as_int(o_[1]), __float_as_int(o_[1]), 0x128, 0xc, 0xf, false)); \
    const float f2_ = __int_as_float(__builtin_amdgcn_update_dpp(__float_as_int(o_[2]), __float_as_int(o_[2]), 0x128, 0xc, 0xf, false)), f3_ = __int_as_float(__builtin_amdgcn_update_dpp(__float_as_int(o_[3]), __float_as_int(o_[3]), 0x128, 0xc, 0xf, false)); \
    a_ = t32(f0_, f2_); b_ = t32(f1_, f3_); } while (0)

constexpr int L_PH = 10240, L_RG = 16384, RG_SLOT = 20480, LAG = RLAG, NRS = LAG + 2;
template <int W>
__device__ __forceinline__ void r_loop(const Args& a, unsigned char* lds, const int u, const unsigned pol, const u32x8 (&F)[4][10]) {
  char* ws = a.ws; asm volatile("" : "+s"(ws));
  unsigned* const cnt = (unsigned*)(ws + O_CNT); unsigned* const tmo = cnt + CNT(C_TMO);
  char* const X1 = ws + O_X1; char* const P = ws + O_PS;
  const int lane = lane_id(), l15 = lane & 15, lq = lane >> 4;
  constexpr bool FIN = W == 0;
  constexpr int wave = W, NT = 4, t0 = 4 * W;
  constexpr int NP = W == 2 ? 16 : 20, NU = W == 1 ? NU1 : (W == 2 ? 8 : 8 - NU1), U0 = W == 1 ? 0 : (W == 2 ? NU1 : NU1 + 8), NL = NP + NU;
  const __amdgpu_buffer_rsrc_t rs_x1 = mkrs(X1), rs_p = mkrs(P);
  int ix = (l15 & 2) ? 0xEEEE : 0x4444; PINV(ix);
  unsigned pt0 = (unsigned)(((l15 >> 3) * 4 + lq) * 128 + (l15 & 7) * 16);
  unsigned pmo = (unsigned)(32768 + (2 * lq + (l15 >> 3)) * 128 + (l15 & 7) * 16);
  unsigned xvo = lane < 8 ? (unsigned)(lane * 16) : 0x80000000u;
  unsigned afb = (unsigned)(L_MEM + lane * 16);
  unsigned ovo = 0x80000000u;
  unsigned tub = (unsigned)(L_RG + lq * 1024 + l15 * 16), zwb = (unsigned)(L_RG + 16384 + t0 * 256 + l15 * 16 + lq * 4);
  unsigned ga = (unsigned)(L_MEM + (lq * 16 + ((lane >> 1) & 3) * 4 + (lane & 1)) * 16 + (lane & 8));
  unsigned oob = 0x80000000u; int tsa = (lane & 31) * 128;
  if (FIN) { PINV(pt0); PINV(pmo); PINV(xvo); PINV(oob); PINV(ga); PINV(tsa); } else { PINV(ovo); PINV(tub); }
  PINV(afb); PINV(zwb);
  const __amdgpu_buffer_rsrc_t rs_uo = mkrs(ws + O_UO + (size_t)u * U_U + U0 * 1024), rs_pt = mkrs(ws + O_PT - LAG * PT_SEG), rs_out = mkrs(a.out + 640 + 8 * u);
  const unsigned* const pop = cnt + CNT(C_PO + u); const unsigned* const tkp = cnt + CNT(C_TOK + u / 20);
  const __amdgpu_buffer_rsrc_t rs_po = mkrs(pop), rs_tk = mkrs(tkp - CNT(4) * (LAG - 1));
  int dlx = a.d[1] + 2 * a.d[4], dlp = a.d[2] + a.d[4];
  const unsigned pso0 = half_of(P, 0u, 0u, pol);
  const __amdgpu_buffer_rsrc_t rs_ts = mkrs(P + pso0 + TS_OFF);
#define TK_P(g_, c_, j_, so_) ld16(rs_pt, afb - L_MEM + (unsigned)((j_) * 1024), (so_) + (unsigned)PT_BLK(g_, c_))
#define PT_AT(i_) half_of(ws + O_PT - LAG * PT_SEG, (unsigned)(i_) * PT_SEG, 0u, pol)
#define TK_A(c_, j_) ((c_) < 4 ? *(const u32x4*)(lds + tua + (j_) * 4096 + (c_) * 256) : *(const u32x4*)(lds + tza + (j_) * 1024))
#define TK_C(n_) ((n_) < 4 ? 4 : ((n_) >> 2) - 1)
#define TK_MM(g_, b_, n0_, n1_, H_) do { u32x4 ta_[8]; \
      _Pragma("unroll") for (int n = (n0_); n < (n0_) + 8 && n < (n1_); ++n) ta_[n & 7] = TK_A(TK_C(n), n & 3); \
      SB(); \
      _Pragma("unroll") for (int n = (n0_); n < (n1_); ++n) { to = mfma(ta_[n & 7], tp[(b_) + n], n == 0 ? ZERO4 : to); asm volatile("" : "+a"(to)); \
        if (n + 8 < (n1_)) ta_[n & 7] = TK_A(TK_C(n + 8), n & 3); SB(); H_; } } while (0)
#define TK_PK(o_) do { float ra, rb; FOLD(to, ra, rb); u32x4 w; ROW16(w, cvtpk(ra, rb), k0_); o_ = (u32x4){w.x, w.z, w.y, w.w}; PINV(o_); } while (0)
#define TK_ST(g_, o_, v_) __builtin_amdgcn_raw_buffer_store_b128(o_, rs_out, (int)(v_), (int)(oso + (unsigned)((g_) * 20480)), 0)
#define P_A(i_) (*(const u32x4*)(lds + upa + (i_) * 1024))
#define PH_SET(k_) do { asm volatile("" ::: "memory"); *(volatile LAS unsigned*)(LAS unsigned char*)(lds + L_PH) = (unsigned)(3 * s + (k_)); asm volatile("" ::: "memory"); } while (0)
#define PH_WAIT(k_) do { unsigned n_ = 0; while ((unsigned)__builtin_amdgcn_readfirstlane(*(volatile LAS unsigned*)(LAS unsigned char*)(lds + L_PH)) < (unsigned)(3 * s + (k_)) && ++n_ < 4096u) __builtin_amdgcn_s_sleep(1); asm volatile("" ::: "memory"); } while (0)
#define P_PASS() do { \
        _Pragma("unroll") for (int i = 0; i < 16; ++i) { p[i] = ld16(rs_p, pt0 + 1024u * (i & 3), (i >> 2) == 0 ? pso : ((i >> 2) == 1 ? ps1 : ((i >> 2) == 2 ? ps2 : ps3))); SB(); } \
        _Pragma("unroll") for (int i = 0; i < 4; ++i) { p[16 + i] = ld16(rs_p, pmo + 1024u * i, pso); SB(); } \
        PH_SET(1); \
        _Pragma("unroll") for (int i = 0; i < NPA; ++i) up[i] = P_A(i); \
        SB(); \
        asm volatile("s_nop 1\n v_mfma_f32_16x16x32_bf16 %0, %1, %2, 0" : "=&{v[252:255]}"(o) : "v"(up[0]), "v"(p[0])); up[0] = P_A(NPA); SB(); \
        _Pragma("unroll") for (int i = 1; i < 19; ++i) { asm volatile("v_mfma_f32_16x16x32_bf16 %0, %1, %2, %0" : "+{v[252:255]}"(o) : "v"(up[i % NPA]), "v"(p[i])); if (i + NPA < 20) up[i] = P_A(i + NPA); SB(); } \
          \
        asm volatile("v_mfma_f32_16x16x32_bf16 %0, %3, %4, %0\n s_nop 7\n" \
                     "v_mov_b32_dpp v252, v252 row_ror:8 row_mask:0xc bank_mask:0xf\n v_mov_b32_dpp v254, v254 row_ror:8 row_mask:0xc bank_mask:0xf\n" \
                     "v_mov_b32_dpp v253, v253 row_ror:8 row_mask:0xc bank_mask:0xf\n v_mov_b32_dpp v255, v255 row_ror:8 row_mask:0xc bank_mask:0xf\n" \
                     "v_permlane32_swap_b32 v252, v254\n v_add_f32 v254, v252, v254\n v_permlane32_swap_b32 v253, v255\n v_add_f32 v255, v253, v255\n" \
                     "v_cmp_u_f32 %1, v254, v255\n v_cvt_pk_bf16_f32 v252, v254, v255\n v_cvt_pk_bf16_f32 v253, v254, v255\n v_cndmask_b32 %2, %5, %6, %1\n s_nop 0\n" \
                     "v_permlane16_swap_b32 v252, v253\n s_nop 1\n v_permlane32_swap_b32 v252, v254\n v_permlane32_swap_b32 v253, v255\n" \
                     "buffer_store_dwordx4 v[252:255], %2, %7, %8 offen sc1\n s_nop 1" \
                     : "+{v[252:255]}"(o), "=&s"(nan), "=&v"(xs) : "v"(up[19 % NPA]), "v"(p[19]), "v"(xvo), "v"(oob), "s"(rs_x1), "s"(xo_u) : "memory"); } while (0)
#define ROW16(w_, pk_, PK2_) do { unsigned k0_ = pk_; asm("" : "+v"(k0_)); float r2_ = ra; asm("" : "+v"(r2_)); const v2u_t c_ = __builtin_amdgcn_permlane16_swap(k0_, PK2_, false, false); \
        const v2u_t d_ = __builtin_amdgcn_permlane32_swap(c_[0], __float_as_uint(ra), false, false), e_ = __builtin_amdgcn_permlane32_swap(c_[1], __float_as_uint(rb), false, false); \
        w_ = (u32x4){d_[0], e_[0], d_[1], e_[1]}; } while (0)
  unsigned rc = 0, rn = RG_SLOT, ro = 2 * RG_SLOT;
  constexpr int NPA = 10;
  f32x4 to;
  u32x4 tp[NP], un[NU > 0 ? NU : 1];
#define R_Z(H_) do { u32x4 f[4]; f[0] = AFR(0); f[1] = AFR(1); f[2] = AFR(2); \
      _Pragma("unroll") for (int k = 0; k < 10; ++k) { \
        if (k + 3 < 10) f[(k + 3) % 4] = AFR(k + 3); \
        _Pragma("unroll") for (int t = 0; t < NT; ++t) { if (t < 3) asm volatile(SMI : "+v"(z[t]) : "v"(f[k % 4]), "a"(F[t][k]), "v"(ix)); else asm volatile(SMI : "+v"(z[t]) : "v"(f[k % 4]), "v"(F[t][k]), "v"(ix)); } \
        H_; SB(); } \
      asm volatile("s_nop 7" : "+v"(z[0]), "+v"(z[1]), "+v"(z[2]), "+v"(z[3])); \
      _Pragma("unroll") for (int t = 0; t < NT; ++t) *(unsigned*)(lds + zwa + t * 256) = cvtpk(nopk(z[t][0] + z[t][2]), nopk(z[t][1] + z[t][3])); \
      SB(); } while (0)
#define RING_ON() do { rc = rn; rn = rn == (NRS - 1) * RG_SLOT ? 0u : rn + RG_SLOT; ro = ro == (NRS - 1) * RG_SLOT ? 0u : ro + RG_SLOT; } while (0)
  if (FIN) {
    const unsigned xu = (unsigned)((u >> 6) * 16384 + (u & 63) * 128);
    unsigned xo = half_of(X1, 0u, 0u, pol), psp = 0;
    u32x4 gv[10];
#define R_GLD() do { unsigned xo2 = xo + 4096u, xo3 = xo + 16384u; _Pragma("unroll") for (int k = 0; k < 10; ++k) { gv[k] = ld16(rs_x1, afb - L_MEM + (unsigned)((k & 3) * 1024), k < 4 ? xo : (k < 8 ? xo2 : xo3)); SB(); } } while (0)
#define R_GST() do { m = 0u; _Pragma("unroll") for (int k = 0; k < 10; ++k) { *(u32x2*)(lds + gx + k * 1024) = (u32x2){gv[k].x, gv[k].y}; *(u32x2*)(lds + gx + k * 1024 + 32) = (u32x2){gv[k].z, gv[k].w}; m = max(m, max(gv[k].x, gv[k].z)); SB(); } } while (0)
#pragma unroll
    for (int k = 0; k < 10; ++k) gv[k] = (u32x4){0u, 0u, 0u, 0u};
    unsigned gx = ga + (unsigned)(L_RG + 4 * RG_SLOT);
#pragma unroll 1
    for (int s = 0;; ++s) {
      f32x4 z[NT];
#pragma unroll
      for (int t = 0; t < NT; ++t) { z[t] = ZERO4; PINV(z[t]); }
      unsigned zwa = zwb + rc, upa = afb + (unsigned)(L_RG - L_MEM) + rc; PINV(zwa); PINV(upa);
      unsigned pso = pso0 + (unsigned)PSLOT(s), xo_u = xo + X1_STEP + xu;
      unsigned ps1 = pso + 4096u, ps2 = pso + 16384u, ps3 = pso + 20480u; asm volatile("" : "+s"(pso), "+s"(ps1), "+s"(ps2), "+s"(ps3), "+s"(xo_u));
      ADAPT(dlp, psp, a.d[2]);
      RING_ON();
      st16(rs_x1, s + PRE + 1 < 64 ? xvo : oob, xo_u + PRE * X1_STEP, (u32x4){~0u, ~0u, ~0u, ~0u});
      SB();
      unsigned m, gsp = 0;
      R_GST();
      if (__builtin_expect(__ballot(m == ~0u) != 0ull, 0)) {
        for (;;) { R_GLD(); R_GST(); const bool go = spin_check(gsp, tmo); if (__ballot(m == ~0u) == 0ull || !go) break; }
      }
      const unsigned tsl = __builtin_amdgcn_raw_buffer_load_b32(rs_ts, tsa, PSLOT(s), 16); SB();
      BAR();
      R_Z((void)0);
      BAR();
      ADAPT(dlx, gsp, a.d[1]);
      unsigned tsv = ts_max(tsl);
      asm volatile("" : "+s"(tsv), "+s"(dlx));
      if (s == 63) break;
      if (__builtin_expect(tsv == ~0u, 0)) tsv = ts_wait(rs_ts, PSLOT(s));
      const unsigned lg0 = wait_rt(tsv + (unsigned)dlp);
      {
        u32x4 p[20], up[NPA], o; u64 nan; unsigned xs;
        P_PASS();
        SB();
        psp = 0;
        if (__builtin_expect((nan & 0x00ff00ff00ff00ffull) != 0ull, 0)) {

          for (;;) {
            P_PASS();
            unsigned bad = 0u;
#pragma unroll
            for (int i = 0; i < 20; ++i) bad |= (p[i].x | p[i].y) | (p[i].z | p[i].w);
            const bool go = spin_check(psp, tmo);
            if (__all((int)bad >= 0) || !go) break;
          }
          st16(rs_x1, xvo, xo_u, (u32x4){min(o.x, 0xfffffffeu), o.y, min(o.z, 0xfffffffeu), o.w});
        }
      }
      PH_SET(2);
      xo += X1_STEP; gx = ga;
      const unsigned tg = wait_rt(tsv + (unsigned)dlx);
      R_GLD();
      PH_SET(3);
    }
    ro = (63 - LAG) % NRS * RG_SLOT;
  } else {
#define R_NEXT(a_, b_, uso, pts) do { _Pragma("unroll") for (int n = (a_); n < (b_); ++n) { const int q = n - NP, n0 = n - 12; \
      if (q >= 0) un[q < 0 ? 0 : q] = ld16(rs_uo, afb - L_MEM + (unsigned)((q & 3) * 1024), uso + (unsigned)((q >> 2) * 4096)); \
      else if (W == 1 && n0 >= 0) tp[q < 0 ? n : 0] = TK_P(0, TK_C(n0), n0 & 3, pts); \
      else tp[q < 0 ? n : 0] = TK_P(W, TK_C(n), n & 3, pts); \
      SB(); } } while (0)
    u32x4 o1, o0; unsigned usp = U_S, ptp = PT_AT(0);
#pragma unroll
    for (int j = 0; j < NP; ++j) tp[j] = (u32x4){0u, 0u, 0u, 0u};
#pragma unroll
    for (int j = 0; j < NU; ++j) un[j] = (u32x4){0u, 0u, 0u, 0u};
#pragma unroll 1
    for (int s = 0;; ++s) {
      f32x4 z[NT];
#pragma unroll
      for (int t = 0; t < NT; ++t) { z[t] = ZERO4; PINV(z[t]); }
      unsigned tkl; u32x4 opl;
      unsigned zwa = zwb + rc, tua = tub + ro, tza = afb + (unsigned)(L_RG - L_MEM + 16384) + ro, uwa = afb + (unsigned)(L_RG - L_MEM + U0 * 1024) + rn; PINV(zwa); PINV(tua); PINV(tza); PINV(uwa);
      int sg = s - LAG; asm volatile("" : "+s"(sg));
      unsigned oso = (unsigned)(sg < 0 ? 0 : sg) * 81920u, uso = (unsigned)(s < 62 ? s + 2 : 63) * U_S, pts = PT_AT(s + 1); asm volatile("" : "+s"(oso), "+s"(uso), "+s"(pts));
      SB();
      BAR();
      R_Z(if (RN3 + k < NL) R_NEXT(RN3 + k, RN3 + k + 1, usp, ptp); if (k == TSK) { opl = ld16(rs_po, 0u, 0u); tkl = __builtin_amdgcn_raw_buffer_load_b32(rs_tk, 0, s * 512, 16); });
      BAR();
      const unsigned ov = ovo;
      if (W == 1) {
        u32x4 ta_[8]; f32x4 t1;
#define TK_C1(n_) TK_C((n_) < 12 ? (n_) : (n_) - 12)
#pragma unroll
        for (int n = 0; n < 8; ++n) ta_[n] = TK_A(TK_C1(n), n & 3);
        SB();
#pragma unroll
        for (int n = 0; n < 20; ++n) {
          if (n < 12) { t1 = mfma(ta_[n & 7], tp[n], n == 0 ? ZERO4 : t1); asm volatile("" : "+a"(t1)); } else { to = mfma(ta_[n & 7], tp[n], n == 12 ? ZERO4 : to); asm volatile("" : "+a"(to)); }
          if (n + 8 < 20) ta_[n & 7] = TK_A(TK_C1(n + 8), n & 3);
          SB(); }
        { float ra, rb, rc_, rd; FOLD(t1, ra, rb); FOLD(to, rc_, rd); u32x4 w; ROW16(w, cvtpk(ra, rb), k0_); o1 = (u32x4){w.x, w.z, w.y, w.w}; ra = rc_; rb = rd; ROW16(w, cvtpk(ra, rb), k0_); o0 = (u32x4){w.x, w.z, w.y, w.w};
          asm volatile("" : "+v"(o1), "+v"(o0)); }
      } else { TK_MM(W, 0, 0, 4 * W + 8, (void)0); TK_PK(o1); }
#define R_OUT() do { TK_ST(W, o1, ov); if (W == 1) TK_ST(0, o0, ov); SB(); } while (0)
      if (s + 1 >= NSELF) {
#pragma unroll
        for (int j = 0; j < NU; ++j) *(u32x4*)(lds + uwa + j * 1024) = un[j];
      }
      unsigned tok = (unsigned)__builtin_amdgcn_readfirstlane(tkl), oprog = (unsigned)__builtin_amdgcn_readfirstlane(min(min(opl.x, opl.y), min(opl.z, opl.w)));
      asm volatile("" : "+s"(tok), "+s"(oprog));
      if (s == 63) break;
      if (__builtin_expect(tok < 32u || oprog < (unsigned)(s + 3) || s < LAG, 0)) {
        if (s == LAG - 1) { const int ln = lane_id(); ovo = ln < 8 ? (unsigned)(ln * 2560) : 0x80000000u; }
        if (s >= LAG - 1 && tok < 32u) wait_ge<4>(tkp + CNT(4) * (s + 1 - LAG), 32u, tmo);
        if (s < 62 && s + 2 >= NSELF && oprog < (unsigned)(s + 3)) wait_u(pop, (unsigned)(s + 3), tmo);
      }
      const unsigned late = (unsigned)__builtin_amdgcn_readfirstlane(*(volatile LAS unsigned*)(LAS unsigned char*)(lds + L_PH)) > (unsigned)(3 * s) ? 1u : 0u;

      PH_WAIT(1 + late); R_NEXT(0, RN1, uso, pts);
      RING_ON();
      PH_WAIT(2 + late); R_NEXT(RN1, RN2, uso, pts);
      if (OPH == 2) R_OUT();
      PH_WAIT(3); R_NEXT(RN2, RN3, uso, pts); usp = uso; ptp = pts;
      if (OPH == 3) R_OUT();
    }
    { const unsigned ov = ovo, oso = (63 - LAG) * 81920u; R_OUT(); }
  }
  if (FIN) { const int ln = lane_id(); ovo = ln < 8 ? (unsigned)(ln * 2560) : 0x80000000u; }
#pragma unroll 1
  for (int sg = 64 - LAG; sg < 64; ++sg) {
    ro = ro == (NRS - 1) * RG_SLOT ? 0u : ro + RG_SLOT;
    wait_ge<1>(tkp + CNT(4) * sg, 32u, tmo);
#pragma unroll
    for (int n = 0; n < 4 * W + 8; ++n) tp[n] = TK_P(W, TK_C(n), n & 3, PT_AT(sg + LAG));
    unsigned tua = tub + ro, tza = afb + (unsigned)(L_RG - L_MEM + 16384) + ro; PINV(tua); PINV(tza);
    const unsigned oso = (unsigned)sg * 81920u;
    u32x4 o1; TK_MM(W, 0, 0, 4 * W + 8, (void)0); TK_PK(o1); TK_ST(W, o1, ovo);
  }
}

__device__ __forceinline__ void r_role(const Args& a, unsigned char* lds, const int u, const int wave) {
  char* ws = a.ws; asm volatile("" : "+s"(ws));
  unsigned* const cnt = (unsigned*)(ws + O_CNT); unsigned* const tmo = cnt + CNT(C_TMO);
  const int lane = lane_id(), l15 = lane & 15, lq = lane >> 4;
  if (wave == 0) *(u32x4*)(lds + L_PH) = (u32x4){0u, 0u, 0u, 0u};
  unsigned xq[2][4];
#pragma unroll
  for (int j = 0; j < 4; ++j) { xq[0][j] = lds_addr(lds) + (unsigned)(40960 + l15 * 1280 + ((4 * j + lq) ^ l15) * 16); xq[1][j] = xq[0][j] + 81920u; PINV(xq[0][j]); PINV(xq[1][j]); }
  if (wave < 2) {
#pragma unroll
    for (int k = 5 * wave, n = 0; n < 5; ++k, ++n) { const u32x4 v = ld16(mkrs(ws + O_X1), (unsigned)lane * 16u, (unsigned)((k & 7) * 1024 + (k >> 3) * 16384));
      unsigned char* d = lds + L_MEM + k * 1024 + (lq * 16 + ((lane >> 1) & 3) * 4 + (lane & 1)) * 16 + (lane & 8); *(u32x2*)d = (u32x2){v.x, v.y}; *(u32x2*)(d + 32) = (u32x2){v.z, v.w}; }
  }
  const unsigned pol = wait_ge<8>(cnt + CNT(C_POL), 1u, tmo) & 1u;
  F_WAIT(u / 20);
  const __amdgpu_buffer_rsrc_t rs_f = mkrs(ws + O_FF + (size_t)(u * 16 + 4 * wave) * 20480);
  u32x4 Fh[4][20]; f32x4 ua[2 * NSELF][4]; u32x4 xa[2 * NSELF];
  constexpr int FW = RFW;
#pragma unroll
  for (int k = 0; k < FW; ++k)
#pragma unroll
    for (int t = 0; t < 4; ++t) { Fh[t][k] = F_LD(t, k); SB(); }
#define R_XF(i_, k_) (*(const LAS u32x4*)(xq[(i_) < 2 ? 1 : 0][(k_) & 3] + ((i_) < 4 ? (i_) : (i_) - 4) * 20480 + ((k_) >> 2) * 256))
#pragma unroll
  for (int i = 0; i < 2 * NSELF; ++i) xa[i] = R_XF(i, 0);
#pragma unroll
  for (int k = 0; k < 20; ++k) {
#pragma unroll
    for (int t = 0; t < 4; ++t) {
#pragma unroll
      for (int i = 0; i < 2 * NSELF; ++i) {
        if (k == 0) { if (t < 3) asm volatile("v_mfma_f32_16x16x32_bf16 %0, %1, %2, 0" : "=&v"(ua[i][t]) : "v"(xa[i]), "a"(Fh[t][k])); else asm volatile("v_mfma_f32_16x16x32_bf16 %0, %1, %2, 0" : "=&v"(ua[i][t]) : "v"(xa[i]), "v"(Fh[t][k])); }
        else { if (t < 3) asm volatile("v_mfma_f32_16x16x32_bf16 %0, %1, %2, %0" : "+v"(ua[i][t]) : "v"(xa[i]), "a"(Fh[t][k])); else asm volatile("v_mfma_f32_16x16x32_bf16 %0, %1, %2, %0" : "+v"(ua[i][t]) : "v"(xa[i]), "v"(Fh[t][k])); }
        if (t == 3 && k + 1 < 20) xa[i] = R_XF(i, k + 1);
        SB(); }
      if (k + FW < 20) { Fh[t][k + FW] = F_LD(t, k + FW); SB(); }
    }
  }
  asm volatile("s_nop 7" : "+v"(ua[2 * NSELF - 3][3]), "+v"(ua[2 * NSELF - 2][3]), "+v"(ua[2 * NSELF - 1][3]));
  BAR();
#pragma unroll
  for (int g = 0; g < NSELF; ++g)
#pragma unroll
    for (int t = 0; t < 4; ++t)
      *(u32x4*)(lds + L_RG + g * RG_SLOT + (4 * wave + t) * 1024 + lane * 16) = (u32x4){cvtpk(ua[2 * g][t][0], ua[2 * g][t][1]), cvtpk(ua[2 * g][t][2], ua[2 * g][t][3]), cvtpk(ua[2 * g + 1][t][0], ua[2 * g + 1][t][1]), cvtpk(ua[2 * g + 1][t][2], ua[2 * g + 1][t][3])};
  u32x8 F[4][10];
#pragma unroll
  for (int t = 0; t < 4; ++t)
#pragma unroll
    for (int k = 0; k < 10; ++k) F[t][k] = cat8(Fh[t][2 * k], Fh[t][2 * k + 1]);
  if (wave == 0) r_loop<0>(a, lds, u, pol, F); else if (wave == 1) r_loop<1>(a, lds, u, pol, F); else if (wave == 2) r_loop<2>(a, lds, u, pol, F); else r_loop<3>(a, lds, u, pol, F);
}

template <bool QK>
__device__ __forceinline__ void g_role(const Args& a, unsigned char* lds, const int u, const int wave, const unsigned tag) {
  char* ws = a.ws; asm volatile("" : "+s"(ws));
  unsigned* const cnt = (unsigned*)(ws + O_CNT); unsigned* const tmo = cnt + CNT(C_TMO);
  const int lane = lane_id(), l15 = lane & 15, lq = lane >> 4;
  u32x8 F[4][10];
  const unsigned rot = QK ? (unsigned)u % 5u : 0u;
  unsigned dvo[10];
#pragma unroll
  for (int i = 0; i < 10; ++i) { const unsigned c = (unsigned)((wave + 4 * i) * 64 + lane), r = c / 80u, pc = c - r * 80u, g = (pc >> 4) + rot;
    dvo[i] = (QK ? r : 8u * ((r & 15u) >> 2) + 4u * (r >> 4) + (r & 3u)) * 1280u + ((g < 5u ? g : g - 5u) * 16u + ((pc ^ r) & 15u)) * 16u; PINV(dvo[i]); }
#define G_XD1(sg_, b_, i_) glds(a.Bx + (size_t)(32 * ((sg_) < 64 ? (sg_) : 63)) * 640, dvo[i_], lds_addr(lds) + (b_) + (unsigned)(wave * 1024 + (i_) * 4096))
#define G_XD(sg_, b_) do { _Pragma("unroll") for (int xi_ = 0; xi_ < 10; ++xi_) G_XD1(sg_, b_, xi_); } while (0)
  if (QK) {
    const char* W_ = (const char*)((u < 24 ? a.Wk : a.Wq) + 256 * (u < 24 ? u : u - 24));
    unsigned svo[7];
#pragma unroll
    for (int q = 0; q < 7; ++q) { const unsigned c = (unsigned)((wave + 4 * q) * 64 + lane), r = c >> 5, pc = c & 31u; svo[q] = r * 12288u + 16u * (pc ^ (((r & 3u) | ((r >> 1) & 4u)) << 1)); PINV(svo[q]); }
    unsigned sv7; { const unsigned c = (unsigned)((wave + 28) * 64 + lane), r = c >> 5, pc = c & 31u; sv7 = r * 12288u + 16u * (pc ^ (((r & 3u) | ((r >> 1) & 4u)) << 1)); PINV(sv7); }
#define G_WD(i_) do { const unsigned g_ = (unsigned)((i_) >> 1) + rot; const char* s_ = W_ + (size_t)((g_ < 5u ? g_ : g_ - 5u) * 128u + ((i_) & 1) * 64u) * 12288; \
      _Pragma("unroll") for (int q = 0; q < 8; ++q) glds(s_, q < 7 ? svo[q < 7 ? q : 0] : sv7, lds_addr(lds) + (unsigned)(((i_) % 3) * 32768 + (wave + 4 * q) * 1024)); SB(); } while (0)
    G_WD(0); G_WD(1); G_XD(0, 122880u); G_WD(2);
    const unsigned row = (unsigned)(8 * lq + (l15 >> 2)), sw = ((row & 3u) | ((row >> 1) & 4u)) << 1;
    unsigned tro[4];
#pragma unroll
    for (int t = 0; t < 4; ++t) { tro[t] = row * 512u + (((unsigned)(2 * (4 * wave + t) + ((l15 & 3) >> 1))) ^ sw) * 16u + (unsigned)(l15 & 1) * 8u; PINV(tro[t]); }
#pragma unroll
    for (int i = 0; i < 10; ++i) {
      if (i == 0) VMW(26); else if (i == 1) VMW(26); else if (i < 8) VMW(16); else if (i == 8) VMW(8); else { init_wait(ws, tag); VMW(0); }
      BAR();
#pragma unroll
      for (int t = 0; t < 4; ++t) F[t][i] = cat8(trrd2(lds, tro[t] + (unsigned)((i % 3) * 32768), 2048u), trrd2(lds, tro[t] + (unsigned)((i % 3) * 32768 + 16384), 2048u));
      BAR();
      if (i + 3 < 10) G_WD(i + 3);
    }
    G_XD(1, 81920u);
  } else {
    F_WAIT(4 + u / 20);
  }
  u32x4 Wf[4][20];
  const __amdgpu_buffer_rsrc_t rs_f = mkrs(ws + O_FF + (size_t)(u * 16 + 4 * wave) * 20480);
#pragma unroll
  for (int k = 0; k < 20; ++k)
#pragma unroll
    for (int t = 0; t < 4; ++t) {
      if (QK) { Wf[t][k] = (k & 1) ? hi4(F[t][k >> 1]) : lo4(F[t][k >> 1]); if (t < 3) asm volatile("" : "+a"(Wf[t][k])); else PINV(Wf[t][k]); }
      else if (k < GFW) { Wf[t][k] = F_LD(t, k); SB(); } }
  const int gw = 4 * (u < 24 ? u : u - 24) + wave, hh = gw / 3, k3 = gw - 3 * hh;
  const __amdgpu_buffer_rsrc_t rs_fl = mkrs(QK ? cnt + CNT(C_PT_ + hh) + (u < 24 ? 0 : 4) + k3 : cnt + CNT(C_PO + u) + wave);
  const __amdgpu_buffer_rsrc_t rs_o = mkrs(QK ? ws + (u < 24 ? O_KF : O_QF) + (size_t)hh * QK_H + k3 * 2048 : ws + O_UO + (size_t)u * U_U + wave * 4096);
  int flo = lane == 0 ? 0 : (int)0x80000000u; PINV(flo);
  constexpr int I0 = QK ? 0 : NSELF;
  unsigned bc = 122880u, bn = 40960u;
  const unsigned lb = lds_addr(lds);
  unsigned xr[4], xq[4], vo = 0x80000000u, fa = lb; PINV(fa);
#pragma unroll
  for (int q = 0; q < 4; ++q) { xr[q] = lb + (unsigned)(l15 * 1280 + ((4 * q + lq) ^ l15) * 16); PINV(xr[q]); xq[q] = xr[q] + bc; }
#define G_XF(rt_, k_) (*(const LAS u32x4*)(xq[(k_) & 3] + (rt_) * 20480 + ((k_) >> 2) * 256))
#define G_MI(bc_, c_, d_, x_, w_) do { if (QK) asm volatile("v_mfma_f32_16x16x32_bf16 %0, %2, %1, " c_ : "+v"(d_) : "v"(x_), bc_(w_)); else asm volatile("v_mfma_f32_16x16x32_bf16 %0, %1, %2, " c_ : "+v"(d_) : "v"(x_), bc_(w_)); } while (0)
#define G_M(k_, t_, rt_) do { if ((t_) < 3) { if ((k_) == 0) G_MI("a", "0", acc[rt_][t_], xa[(k_) & 3][rt_], Wf[t_][k_]); else G_MI("a", "%0", acc[rt_][t_], xa[(k_) & 3][rt_], Wf[t_][k_]); } \
      else { if ((k_) == 0) G_MI("v", "0", acc[rt_][t_], xa[(k_) & 3][rt_], Wf[t_][k_]); else G_MI("v", "%0", acc[rt_][t_], xa[(k_) & 3][rt_], Wf[t_][k_]); } SB(); } while (0)
#define G_PK(c_) do { const f32x4 v_ = acc[((c_) >> 1) & 1][(c_) >> 2]; const int sj = QK ? ((c_) & 2) + ((c_) >> 3) : (c_) >> 2, se = QK ? (((c_) >> 1) & 2) + ((c_) & 1) : (c_) & 3; \
      pk[sj][se] = ((c_) & 1) ? cvtpk(v_[2], v_[3]) : cvtpk(v_[0], v_[1]); PINV(pk[sj]); SB(); } while (0)
#define G_ST(j_, i_) do { \
      st16(rs_o, vo, (unsigned)(i_) * (QK ? QK_S : U_S) + (unsigned)(QK ? ((j_) >> 1) * 6144 + ((j_) & 1) * 1024 : (j_) * 1024), pk[j_]); SB(); } while (0)
  f32x4 acc[2][4]; u32x4 xa[4][2], pk[4]; unsigned m0k, fw = lb + (unsigned)wave * 4u; PINV(fw);
#pragma unroll
  for (int j = 0; j < 8; ++j) { acc[j >> 2][j & 3] = ZERO4; PINV(acc[j >> 2][j & 3]); }
#pragma unroll
  for (int c = 0; c < 4; ++c) pk[c] = (u32x4){0u, 0u, 0u, 0u};
  *(volatile LAS unsigned*)fw = 0u;
  if (QK) VMW(10);
  BAR();
  asm volatile("s_mov_b32 %0, m0" : "=s"(m0k));
#pragma unroll
  for (int k = 0; k < 2; ++k) { xa[k][0] = G_XF(0, k); xa[k][1] = G_XF(1, k); }
#define G_K(FIRST_, k_) { constexpr int k = k_; \
      _Pragma("unroll") for (int j = 0; j < 8; ++j) { \
        const int t = j >> 1, rt = j & 1, gp = k == 19 ? j : (k == 0 ? 8 + j : -1), dq = 2 * (k - 12) + (j == 6); const bool dm = k >= 12 && k < 17 && (j == 3 || j == 6); \
        if (dm) asm volatile("s_mov_b32 m0, %0" :: "s"(md + (unsigned)(dq * 4096))); \
        G_M(k, t, rt); \
        if (FIRST_ && rt == 1 && k + GFW < 20) { Wf[t][k + GFW < 20 ? k + GFW : 0] = F_LD(t, k + GFW); SB(); } \
        if (j < 2) { xa[(k + 2) & 3][j] = G_XF(j, (k + 2) % 20); SB(); } \
        _Pragma("unroll") for (int c = 0; c < 16; ++c) if (gp == 3 + c * 3 / 4) G_PK(c); \
        if (k % 3 == 1 && k < 12 && j == 5) G_ST(k / 3, ip);        \
        if (k == 2 && j == 3) { xo_ = (unsigned)min(i + 2, 63) * 40960u; asm volatile("" : "+s"(xo_)); SB(); } \
        if (k == 2 && j == 4) { xs = (const u16*)((const char*)a.Bx + xo_); asm volatile("" : "+s"(xs)); SB(); } \
        if (k == 2 && j == 5) { md = lb + bn + (unsigned)wave * 1024u; asm volatile("" : "+s"(md)); bn = bc; SB(); } \
        if (k == 2 && j == 6) { bc = bc == 40960u ? 122880u : bc - 40960u; asm volatile("" : "+s"(bc)); SB(); } \
        if (k == 3 && j == 3) VV(iv, (unsigned)(i + 1)); \
        if (k == 3 && j == 5) VV(iw, (unsigned)i); \
        if (k == 6 && j == 3) { if (FIRST_) ; else VMW(2); *(volatile LAS unsigned*)fw = iv; SB(); } \
        if (k == 10 && j == 3) { fg = *(const volatile LAS u32x4*)fa; SB(); } \
        if (k == 11 && j == 2) VV(mn, min(fg.x, fg.y)); \
        if (k == 11 && j == 3) VV(mn, min3u(mn, fg.z, fg.w)); \
        if (k == 11 && j == 4) { \
          if (__builtin_expect(__ballot(mn < iv) != 0ull, 0)) { int n_ = 0; do { fg = *(const volatile LAS u32x4*)fa; mn = min(min3u(fg.x, fg.y, fg.z), fg.w); } while (__ballot(mn < iv) != 0ull && ++n_ < (1 << 22)); } \
          asm volatile("" ::: "memory"); SB(); } \
        if (dm) { asm volatile("global_load_lds_dwordx4 %0, %1" :: "v"(dvo[dm ? dq : 0]), "s"(xs) : "memory"); SB(); } \
        if (k == 17 && j == 2) { VMW(10); __builtin_amdgcn_raw_buffer_store_b32(iw, rs_fl, flo, 0, 16); SB(); }        \
        if (k == 17 && j >= 4) { xq[j - 4] = xr[j - 4] + bc; PINV(xq[j - 4]); SB(); } \
      } }
#define G_K4(F_, k_) G_K(F_, k_) G_K(F_, k_ + 1) G_K(F_, k_ + 2) G_K(F_, k_ + 3)
#define G_SEG(F_) do { const u16* xs; unsigned xo_, md, iv, iw, mn; u32x4 fg; G_K4(F_, 0) G_K4(F_, 4) G_K4(F_, 8) G_K4(F_, 12) G_K4(F_, 16) } while (0)
#define G_FLUSH(i_) do { asm volatile("s_nop 7" : "+v"(acc[1][1]), "+v"(acc[0][2]), "+v"(acc[1][2]), "+v"(acc[0][3]), "+v"(acc[1][3])); \
      _Pragma("unroll") for (int c = 7; c < 16; ++c) G_PK(c); \
      _Pragma("unroll") for (int j = 0; j < 4; ++j) G_ST(j, i_); } while (0)
#define G_FIRST() do { vo = (unsigned)lane_id() * 16u; G_FLUSH(I0); VMW(0); __builtin_amdgcn_raw_buffer_store_b32(I0 + 1u, rs_fl, flo, 0, 16); } while (0)
  if (!QK) { const int i = I0, ip = I0; G_SEG(true); G_FIRST(); }
#pragma unroll 1
  for (int i = QK ? I0 : I0 + 1, ip = I0; i < 64; ip = i, ++i) {
    G_SEG(false);
    if (__builtin_expect(i < I0 + (QK ? AQ0 + APN : AU0 + APU), 0)) {
      if (i == I0) G_FIRST();
      else if (i >= I0 + (QK ? AQ0 : AU0)) {
        const int n = ((QK ? (i - AQ0) * 48 : APN * 48 + (i - I0 - AU0) * 80) + u) * 32768 + wave * 8192;
        const char* src = n < 2457600 ? (const char*)a.Awqkv + n : (n < 3276800 ? (const char*)a.Awo + (n - 2457600) : (const char*)a.Ax + (n < 5898240 ? n - 3276800 : 0));
        touch(src + lane_id() * 128, lb + 256u); }
    }
  }
  G_FLUSH(63);
  asm volatile("s_mov_b32 m0, %0" :: "s"(m0k));
  VMW(0);
  __builtin_amdgcn_raw_buffer_store_b32(64u, rs_fl, flo, 0, 16);
  BAR();
}

__device__ __forceinline__ void att_role(const Args& a, const int h, const int par) {
  char* ws = a.ws;
  unsigned* const cnt = (unsigned*)(ws + O_CNT); unsigned* const tmo = cnt + CNT(C_TMO);
  const int lane = lane_id(), l15 = lane & 15, lq = lane >> 4;
  const __amdgpu_buffer_rsrc_t rs_q = mkrs(ws + O_QF + (size_t)h * QK_H), rs_k = mkrs(ws + O_KF + (size_t)h * QK_H), rs_km = mkrs(ws + O_KM + (size_t)h * KM_H), rs_pt = mkrs(ws + O_PT + (size_t)h * 128);
  const unsigned pol = wait_ge<8>(cnt + CNT(C_POL), 1u, tmo) & 1u;
  unsigned pv[5];
  { const int gl = l15 >> 3, cl = lq >> 1; const unsigned ro = (unsigned)((l15 & 7) * 16 + (lq & 1) * 8);
    pv[0] = cl <= gl ? (unsigned)PT_BLK(gl, cl) + ro : 0x80000000u; pv[1] = (unsigned)PT_BLK(2 + gl, cl) + ro; pv[2] = cl <= gl ? (unsigned)PT_BLK(2 + gl, 2 + cl) + ro : 0x80000000u;
    pv[3] = lq < 2 ? (unsigned)PT_BLK(gl, 4) + ro : 0x80000000u; pv[4] = lq < 2 ? (unsigned)PT_BLK(2 + gl, 4) + ro : 0x80000000u; }
  const float scale = 0.07216878364870323f, LOG2E = 1.4426950408889634f;
  unsigned tprog = 0;
  u32x4 qn[12], kn[12];
#define AT_LD(s_) do { if (tprog < (unsigned)((s_) + 1)) tprog = wait_t<16>(cnt + CNT(C_PT_ + h), true, (unsigned)((s_) + 1), tmo); \
    _Pragma("unroll") for (int i = 0; i < 12; ++i) { kn[i] = ld16(rs_k, (unsigned)(i * 1024 + lane * 16), (unsigned)(s_) * QK_S); qn[i] = ld16(rs_q, (unsigned)(i * 1024 + lane * 16), (unsigned)(s_) * QK_S); } SB(); } while (0)
  AT_LD(par);
#pragma unroll 1
  for (int s = par; s < 64; s += 2) {
    f32x4 sc[2][3];
    u32x4 qf[2][6];
    float mx[2], sum[2];
#pragma unroll
    for (int q = 0; q < 2; ++q)
#pragma unroll
      for (int t = 0; t <= q; ++t) {
        sc[q][t] = ZERO4;
#pragma unroll
        for (int kk = 0; kk < 6; ++kk) sc[q][t] = mfma(kn[6 * t + kk], qn[6 * q + kk], sc[q][t]);
      }
#pragma unroll
    for (int i = 0; i < 12; ++i) { qf[i / 6][i % 6] = qn[i]; PINV(qf[i / 6][i % 6]); }
    asm volatile("" : "+a"(sc[0][0]), "+a"(sc[1][0]), "+a"(sc[1][1])); SB();
    AT_LD(s < 62 ? s + 2 : s);
#pragma unroll
    for (int q = 0; q < 2; ++q) {
      mx[q] = -3.0e38f;
#pragma unroll
      for (int t = 0; t <= q; ++t)
#pragma unroll
        for (int r = 0; r < 4; ++r) {
          const float v = rbf(rbf(sc[q][t][r]) * scale);
          sc[q][t][r] = (t < q || 4 * lq + r <= l15) ? v : -3.0e38f;
          mx[q] = fmaxf(mx[q], sc[q][t][r]);
        }
      mx[q] = max32(max16(mx[q]));
      sum[q] = 0.f;
#pragma unroll
      for (int t = 0; t <= q; ++t)
#pragma unroll
        for (int r = 0; r < 4; ++r) { const float e = (t < q || 4 * lq + r <= l15) ? __builtin_amdgcn_exp2f(nopk(sc[q][t][r] - mx[q]) * LOG2E) : 0.f; sc[q][t][r] = e; sum[q] += e; }
      PINV(mx[q]); PINV(sum[q]); PINV(sc[q][0]);
    }
    PINV(sc[1][1]);
    SB();
    if (s >= 60) wait_ge<2>(cnt + CNT(C_PS + h), (unsigned)(s == 63 ? 63 : s + 1), tmo); else wait_ge<6>(cnt + CNT(C_PS + h), (unsigned)(s + 1), tmo);
    {
      u32x4 km[6];
      unsigned spins = 0;
      for (;;) {
        unsigned m = 0u;
#pragma unroll
        for (int kk = 0; kk < 6; ++kk) { km[kk] = ld16(rs_km, (unsigned)(kk * 512 + lq * 128 + (l15 & 7) * 16), (unsigned)s * KM_S); m = max(m, max(km[kk].x, km[kk].z)); }
        if (__all(m != ~0u)) break;
        __builtin_amdgcn_s_sleep(3);
        if (!spin_check(spins, tmo)) break;
      }
#pragma unroll
      for (int q = 0; q < 2; ++q) {
        sc[q][2] = ZERO4;
#pragma unroll
        for (int kk = 0; kk < 6; ++kk) sc[q][2] = mfma(km[kk], qf[q][kk], sc[q][2]);
      }
    }
    const unsigned po = half_of(ws + O_PT, (unsigned)s * PT_SEG, 0u, pol);
#pragma unroll
    for (int q = 0; q < 2; ++q) {
      float m = mx[q];
#pragma unroll
      for (int r = 0; r < 4; ++r) { sc[q][2][r] = rbf(rbf(sc[q][2][r]) * scale); m = fmaxf(m, sc[q][2][r]); }
      m = max16(m);
      const float f = __builtin_amdgcn_exp2f(nopk(mx[q] - m) * LOG2E);
      float sm = 0.f;
#pragma unroll
      for (int r = 0; r < 4; ++r) { sc[q][2][r] = __builtin_amdgcn_exp2f(nopk(sc[q][2][r] - m) * LOG2E); sm += sc[q][2][r]; }
      const float inv = __builtin_amdgcn_rcpf(sum32(sum16(__builtin_fmaf(sm, 0.5f, sum[q] * f)))), gg = f * inv;
      st8(rs_pt, pv[q], po, cvtpk(sc[q][0][0] * gg, sc[q][0][1] * gg), cvtpk(sc[q][0][2] * gg, sc[q][0][3] * gg));
      if (q == 1) st8(rs_pt, pv[2], po, cvtpk(sc[1][1][0] * gg, sc[1][1][1] * gg), cvtpk(sc[1][1][2] * gg, sc[1][1][3] * gg));
      st8(rs_pt, pv[3 + q], po, cvtpk(sc[q][2][0] * inv, sc[q][2][1] * inv), cvtpk(sc[q][2][2] * inv, sc[q][2][3] * inv));
    }
    VMW(0);
    if (lane < 4) __hip_atomic_fetch_add((gu32*)(cnt + CNT(C_TOK + 4 * s + lane)), 1u, RLX_AGENT);
  }
}

__device__ __forceinline__ float dot2bf(unsigned x, unsigned y, float c) { return __builtin_amdgcn_fdot2_f32_bf16(__builtin_bit_cast(bf16x2_t, x), __builtin_bit_cast(bf16x2_t, y), c, false); }
__device__ __forceinline__ void a_role(const Args& a, unsigned char* lds, const int j, const int rg, const int wave) {
  char* ws = a.ws; asm volatile("" : "+s"(ws));
  unsigned* const cnt = (unsigned*)(ws + O_CNT); unsigned* const tmo = cnt + CNT(C_TMO);
  const int lane = lane_id(), l15 = lane & 15, lq = lane >> 4;
  const unsigned lb = lds_addr(lds);
  constexpr int LR = 81920, RS = 288;
  const int r0 = 256 * rg - 16;
  {
    u32x4 wt[2][20];
    {
      const unsigned r = (unsigned)(4 * wave + lq), p0 = (unsigned)l15 ^ (((r & 3u) | ((r >> 1) & 4u)) << 1), p = p0 < 15u ? p0 : 14u;
      unsigned wv = r * 3840u + (p / 5u) * 1280u + (p % 5u) * 16u; PINV(wv);
#pragma unroll
      for (int q = 0; q < 40; ++q) glds(a.Awqkv + 40 * j + q * (16 * 1920), wv, lb + (unsigned)((wave + 4 * q) * 1024));
    }
    unsigned dvo[5];
#pragma unroll
    for (int i = 0; i < 5; ++i) { const unsigned c = (unsigned)((wave + 4 * i) * 64 + lane), r = c / 80u; dvo[i] = (c ^ r) * 16u; PINV(dvo[i]); }
#define A_XS(t_) (a.Ax + (size_t)max(r0 + 16 * min(t_, 16), 0) * 640)
#define A_XD1(src_, t_, i_) glds(src_, dvo[i_], lb + (unsigned)(((t_) & 3) * 20480 + wave * 1024 + (i_) * 4096))
    const unsigned row = (unsigned)(8 * lq + (l15 >> 2)), sw = ((row & 3u) | ((row >> 1) & 4u)) << 1;
    unsigned tro[2];
#pragma unroll
    for (int t = 0; t < 2; ++t) tro[t] = row * 256u + (((unsigned)(2 * (2 * wave + t) + ((l15 & 3) >> 1))) ^ sw) * 16u + (unsigned)(l15 & 1) * 8u;
#pragma unroll
    for (int i = 0; i < 5; ++i) {
      if (i == 0) VMW(32); else if (i == 1) VMW(24); else if (i == 2) VMW(16); else if (i == 3) VMW(8); else VMW(15);
      BAR();
      if (i == 3) {
#pragma unroll
        for (int q = 0; q < 15; ++q) A_XD1(A_XS(q / 5), q / 5, q % 5);
      }
#pragma unroll
      for (int t = 0; t < 2; ++t) { unsigned o_ = tro[t] + (unsigned)(i * 32768); PINV(o_);
#pragma unroll
        for (int k = 0; k < 4; ++k) wt[t][4 * i + k] = trrd2(lds, o_ + (unsigned)(k * 8192), 1024u); }
      SB();
    }
    unsigned xr[4], xq[4], wo_[2];
#pragma unroll
    for (int q = 0; q < 4; ++q) { xr[q] = lb + (unsigned)(l15 * 1280 + ((4 * q + lq) ^ l15) * 16); PINV(xr[q]); xq[q] = xr[q]; }
#pragma unroll
    for (int t = 0; t < 2; ++t) { const int v = 8 * wave + 4 * t + lq; wo_[t] = lb + (unsigned)(LR + l15 * RS + (v < 30 ? (v / 5) * 48 + (v % 5) * 8 : (v - 30) * 48 + 40)); }
    unsigned fa = lb + 163824u, fw = fa + (unsigned)wave * 4u; PINV(fa); PINV(fw);
    BAR();
    *(volatile LAS unsigned*)fw = 0u;
    VMW(10);
    BAR();
#define A_XF(k_) (*(const LAS u32x4*)(xq[(k_) & 3] + ((k_) >> 2) * 256))
    f32x4 acc[2][2] = {{ZERO4, ZERO4}, {ZERO4, ZERO4}}; u32x4 xa[4];
#pragma unroll
    for (int k = 0; k < 3; ++k) xa[k] = A_XF(k);
#define A_TILE(T_, P_) do { const u16* xs = A_XS((T_) + 3); unsigned md = lb + (unsigned)((((T_) + 3) & 3) * 20480 + wave * 1024), iv, mn; asm volatile("" : "+s"(xs)); asm volatile("" : "+s"(md)); u32x2 pw[2]; u32x4 fg; \
      _Pragma("unroll") for (int k = 0; k < 20; ++k) { \
        _Pragma("unroll") for (int t = 0; t < 2; ++t) { const bool dm = t == 1 && k >= 11 && k < 16; \
          if (dm) asm volatile("s_mov_b32 m0, %0" :: "s"(md + (unsigned)((k - 11) * 4096))); \
          if (k == 0) asm volatile("v_mfma_f32_16x16x32_bf16 %0, %1, %2, 0" : "=&v"(acc[P_][t]) : "v"(wt[t][k]), "v"(xa[k & 3])); \
          else asm volatile("v_mfma_f32_16x16x32_bf16 %0, %1, %2, %0" : "+v"(acc[P_][t]) : "v"(wt[t][k]), "v"(xa[k & 3])); \
          SB(); \
          if (t == 0) { xa[(k + 3) & 3] = A_XF((k + 3) % 20); SB(); } \
          if (t == 1 && k == 0) VV(iv, (unsigned)((T_) + 1)); \
          if (t == 1 && k == 1) { VMW(5); *(volatile LAS unsigned*)fw = iv; SB(); }        \
          if (t == 1 && (k == 2 || k == 3)) { pw[k & 1] = (u32x2){cvtpk(acc[1 - (P_)][k & 1][0], acc[1 - (P_)][k & 1][1]), cvtpk(acc[1 - (P_)][k & 1][2], acc[1 - (P_)][k & 1][3])}; PINV(pw[k & 1]); SB(); } \
          if (t == 1 && k == 4) { *(LAS u32x2*)wo_[0] = pw[0]; *(LAS u32x2*)wo_[1] = pw[1]; SB(); } \
          if (t == 1 && k == 5) { wo_[0] += wst; wo_[1] += wst; wst = 16 * RS; PINV(wo_[0]); PINV(wo_[1]); SB(); } \
          if (t == 1 && k == 6) { fg = *(const volatile LAS u32x4*)fa; SB(); } \
          if (t == 1 && k == 9) VV(mn, min(min3u(fg.x, fg.y, fg.z), fg.w)); \
          if (t == 1 && k == 10) { \
            if (__builtin_expect(__ballot(mn < iv) != 0ull, 0)) { int n_ = 0; do { fg = *(const volatile LAS u32x4*)fa; mn = min(min3u(fg.x, fg.y, fg.z), fg.w); } while (__ballot(mn < iv) != 0ull && ++n_ < (1 << 22)); } \
            asm volatile("" ::: "memory"); SB(); } \
          if (dm) { asm volatile("global_load_lds_dwordx4 %0, %1" :: "v"(dvo[dm ? k - 11 : 0]), "s"(xs) : "memory"); SB(); } \
          if (t == 0 && k >= 13 && k < 17) { xq[(k + 3) & 3] = xr[(k + 3) & 3] + (unsigned)((((T_) + 1) & 3) * 20480); PINV(xq[(k + 3) & 3]); SB(); } \
        } } } while (0)
    unsigned m0k; asm volatile("s_mov_b32 %0, m0" : "=s"(m0k));
    unsigned wst = 0;
#pragma unroll 1
    for (int T = 0; T < 16; T += 2) { A_TILE(T, 0); A_TILE(T + 1, 1); }
    A_TILE(16, 0);
    asm volatile("s_mov_b32 m0, %0" :: "s"(m0k));
    asm volatile("s_nop 7" : "+v"(acc[0][0]), "+v"(acc[0][1]));
#pragma unroll
    for (int t = 0; t < 2; ++t) *(LAS u32x2*)wo_[t] = (u32x2){cvtpk(acc[0][t][0], acc[0][t][1]), cvtpk(acc[0][t][2], acc[0][t][3])};
  }
  VMW(0);
  BAR();
  const int jc = j & 7;
  unsigned ov_[5];
#pragma unroll
  for (int i = 0; i < 5; ++i) { const unsigned c = (unsigned)((wave + 4 * i) * 64 + lane), r = c / 10u; ov_[i] = r * 1280u + (c - r * 10u) * 16u; PINV(ov_[i]); }
#define A_OD(g_) do { _Pragma("unroll") for (int i = 0; i < 5; ++i) glds(a.Awo + 80 * jc + (g_) * (128 * 640), ov_[i], lb + (unsigned)((g_) * 20480 + (wave + 4 * i) * 1024)); } while (0)
  A_OD(0); A_OD(1); A_OD(2); A_OD(3);
  const __amdgpu_buffer_rsrc_t rs_oa = mkrs(ws + O_OA);
#pragma unroll 1
  for (int ps = 0; ps < 2; ++ps) {
    const int rl = 32 * (2 * wave + ps) + (lane & 31), row = 256 * rg + rl, hh = lane >> 5;
    unsigned ra[8];
#pragma unroll
    for (int w = 0; w < 8; ++w) ra[w] = (unsigned)(LR + 48 * hh + (rl + 16 - 2 * w) * RS);
#define A_LD10(d_, ad_) do { const u32x4 x0 = *(const u32x4*)(lds + (ad_)), x1 = *(const u32x4*)(lds + (ad_) + 16); const u32x2 x2 = *(const u32x2*)(lds + (ad_) + 32); \
        d_[0] = x0.x; d_[1] = x0.y; d_[2] = x0.z; d_[3] = x0.w; d_[4] = x1.x; d_[5] = x1.y; d_[6] = x1.z; d_[7] = x1.w; d_[8] = x2.x; d_[9] = x2.y; } while (0)
      unsigned q[10], kv[8][10], vv[8][10];
      A_LD10(q, ra[0]);
#pragma unroll
      for (int w = 0; w < 8; ++w) A_LD10(kv[w], ra[w] + 96);
#pragma unroll
      for (int w = 0; w < 8; ++w) A_LD10(vv[w], ra[w] + 192);
      SB();
      float lg[8], mx = -3.0e38f;
#pragma unroll
      for (int w = 0; w < 8; ++w) lg[w] = 0.f;
#pragma unroll
      for (int i = 0; i < 10; ++i)
#pragma unroll
        for (int w = 0; w < 8; ++w) lg[w] = dot2bf(q[i], kv[w][i], lg[w]);
#pragma unroll
      for (int w = 0; w < 8; ++w) { lg[w] = row - 2 * w >= 0 ? rbf(rbf(lg[w]) * 0.22360679774997896f) : rbf(-1000000000.0f); mx = fmaxf(mx, lg[w]); }
      float sum = 0.f;
#pragma unroll
      for (int w = 0; w < 8; ++w) { lg[w] = __expf(lg[w] - mx); sum += lg[w]; }
      const float inv = __builtin_amdgcn_rcpf(sum);
      float ov[20];
#pragma unroll
      for (int d = 0; d < 20; ++d) ov[d] = 0.f;
#pragma unroll
      for (int w = 0; w < 8; w += 2) {
        const unsigned pw = cvtpk(lg[w] * inv, lg[w + 1] * inv);
#pragma unroll
        for (int i = 0; i < 10; ++i) {
          ov[2 * i] = dot2bf(__builtin_amdgcn_perm(vv[w + 1][i], vv[w][i], 0x05040100u), pw, ov[2 * i]);
          ov[2 * i + 1] = dot2bf(__builtin_amdgcn_perm(vv[w + 1][i], vv[w][i], 0x07060302u), pw, ov[2 * i + 1]);
        }
      }
    unsigned pk[10];
#pragma unroll
    for (int i = 0; i < 10; ++i) pk[i] = cvtpk(ov[2 * i], ov[2 * i + 1]);
    const v2u_t m0 = __builtin_amdgcn_permlane32_swap(pk[0], pk[0], false, false), m1 = __builtin_amdgcn_permlane32_swap(pk[1], pk[1], false, false);
    const unsigned oo = (unsigned)((row >> 4) * 20480 + (row & 15) * 16 + (5 * j + 3 * hh) * 256);
    st16(rs_oa, oo, 0u, hh ? (u32x4){pk[2], pk[3], pk[4], pk[5]} : (u32x4){pk[0], pk[1], pk[2], pk[3]});
    st16(rs_oa, oo + 256u, 0u, hh ? (u32x4){pk[6], pk[7], pk[8], pk[9]} : (u32x4){pk[4], pk[5], pk[6], pk[7]});
    st16(rs_oa, hh ? 0x80000000u : oo + 512u, 0u, (u32x4){pk[8], pk[9], m0[1], m1[1]});
  }
  VMW(0);
  BAR();
  if (wave == 3 && lane == 0) __hip_atomic_fetch_add((gu32*)(cnt + CNT(C_AO + rg)), 1u, RLX_AGENT);
  A_OD(4);
  u32x4 wt[5][20];
  {
    unsigned o_ = (unsigned)((8 * lq + (l15 >> 2)) * 160 + 8 * (l15 & 3)), o2 = o_ + 51200u; PINV(o_); PINV(o2);
#pragma unroll
    for (int kk = 0; kk < 20; ++kk) {
      if (kk == 16) { VMW(0); BAR(); }
#pragma unroll
      for (int t = 0; t < 5; ++t) wt[t][kk] = trrd2(lds, (kk < 10 ? o_ : o2) + (unsigned)((kk % 10) * 5120 + 32 * t), 640u);
      SB();
    }
  }
  wait_ge<4>(cnt + CNT(C_AO + rg), 16u, tmo);
  const int rt0 = 16 * rg + 8 * (j >> 3) + 2 * wave;
  const __amdgpu_buffer_rsrc_t rs_of = mkrs(ws + O_OA + (size_t)rt0 * 20480), rs_out = mkrs(a.out + (size_t)rt0 * (16 * 1280) + 80 * jc);
  unsigned fo = (unsigned)lane * 16u; PINV(fo);
  u32x4 of[16];
#pragma unroll
  for (int n = 0; n < 16; ++n) { of[n] = ld16(rs_of, fo, (unsigned)(n * 1024)); SB(); }
  f32x4 acc[5];
#pragma unroll
  for (int n = 0; n < 40; ++n) {
    const int k = n % 20;
#pragma unroll
    for (int t = 0; t < 5; ++t) {
      if (k == 0) { if (t < 4) asm volatile("v_mfma_f32_16x16x32_bf16 %0, %1, %2, 0" : "=&v"(acc[t]) : "a"(wt[t][k]), "v"(of[n & 15])); else asm volatile("v_mfma_f32_16x16x32_bf16 %0, %1, %2, 0" : "=&v"(acc[t]) : "v"(wt[t][k]), "v"(of[n & 15])); }
      else { if (t < 3 || (t == 3 && k < 3)) asm volatile("v_mfma_f32_16x16x32_bf16 %0, %1, %2, %0" : "+v"(acc[t]) : "a"(wt[t][k]), "v"(of[n & 15])); else asm volatile("v_mfma_f32_16x16x32_bf16 %0, %1, %2, %0" : "+v"(acc[t]) : "v"(wt[t][k]), "v"(of[n & 15])); }
    }
    SB();
    if (n + 16 < 40) { of[n & 15] = ld16(rs_of, fo, (unsigned)((n + 16) * 1024)); SB(); }
    if (k == 19) {
      asm volatile("s_nop 7" : "+v"(acc[0]), "+v"(acc[1]), "+v"(acc[2]), "+v"(acc[3]), "+v"(acc[4]));
      const int ln = lane_id();
#pragma unroll
      for (int t = 0; t < 5; ++t) { const u32x2 w = {cvtpk(acc[t][0], acc[t][1]), cvtpk(acc[t][2], acc[t][3])}; __builtin_amdgcn_raw_buffer_store_b64(w, rs_out, (ln & 15) * 2560 + (ln >> 4) * 8 + 32 * t, (n / 20) * (16 * 2560), 0); }
      SB();
    }
  }
}

__device__ __forceinline__ void calibrate(char* ws, const u16* mem0, const int id, const unsigned tag) {
  unsigned* const cnt = (unsigned*)(ws + O_CNT);
  const int lane = lane_id();
  if (id < 20) {
    const int k = id >> 1;
    const u32x4 v = *(const u32x4*)(mem0 + (lane & 7) * 640 + 64 * k + (lane >> 3) * 8);
    st16(mkrs(ws + O_X1), (unsigned)(lane * 16), (unsigned)((k & 7) * 1024 + (k >> 3) * 16384 + (id & 1) * 8192), (u32x4){min(v.x, 0xfffffffeu), v.z, min(v.y, 0xfffffffeu), v.w});
  }
  {
    const u32x4 ones = {~0u, ~0u, ~0u, ~0u}; const unsigned vo = (unsigned)lane * 16u;
    if (id < 60) st16(mkrs(cnt), vo, (unsigned)id * 1024u, (u32x4){0u, 0u, 0u, 0u});
    { const unsigned ko = 63u * KM_S + ((unsigned)id >> 1) * KM_H + ((unsigned)id & 1u) * 1024u; st16(mkrs(ws + O_KM), vo, ko, ones); st16(mkrs(ws + O_KM), vo, ko + 1024u, ones); }
    const unsigned sl = (unsigned)id >> 3, e = (unsigned)id & 7u;
    static_assert(PRE == 8, "");
#pragma unroll
    for (int i = 0; i < 6; ++i) st16(mkrs(ws + O_PS), vo, sl * 65536u + (e * 6u + (unsigned)i) * 1024u, ones);
#pragma unroll
    for (int i = 0; i < 2; ++i) st16(mkrs(ws + O_X1), vo, (sl + 1u) * X1_STEP + (e * 2u + (unsigned)i) * 1024u, ones);
    if (e < 4u) st16(mkrs(ws + O_X1), vo, (sl + 1u) * X1_STEP + 16384u + (e >> 1) * 8192u + (e & 1u) * 1024u, ones);
    VMW(0);
    if (lane == 0) __hip_atomic_store((gu32*)(cnt + W_DONE + id), tag, RLX_AGENT);
  }
  char* cal = ws + O_CAL + (id & 3) * 16384 + (id >> 2) * 512;
  const unsigned flip = ((unsigned)((size_t)cal >> 21) ^ xcc_side()) & 1u;
  unsigned best[2] = {65535u, 65535u}, acc = 0;
#pragma unroll 1
  for (int r = (id & 4) ? CALN : 0; r < CALN; ++r) {
#pragma unroll
    for (int hf = 0; hf < 2; ++hf) {
      const unsigned long long t0 = __builtin_amdgcn_s_memtime();
      if (lane == 0) acc += __hip_atomic_fetch_add((gu32*)(cal + ((unsigned)hf ^ flip) * 8192u + (acc & 0u)), 1u, RLX_AGENT);
      asm volatile("s_waitcnt vmcnt(0)" ::: "memory");
      const unsigned d = (unsigned)(__builtin_amdgcn_s_memtime() - t0);
      best[hf] = min(best[hf], d);
    }
  }
  init_wait(ws, tag);
  if (lane < 2) __hip_atomic_fetch_max((gu32*)(cnt + CNT(C_FILL) + lane), 65536u - (lane ? best[1] : best[0]) + (acc & 0u), RLX_AGENT);
  VMW(0);
  const unsigned n = (unsigned)__builtin_amdgcn_readfirstlane(__hip_atomic_fetch_add((gu32*)(cnt + CNT(C_FILL) + 2), lane == 0 ? 1u : 0u, RLX_AGENT));
  if (n == 63u) {
    const u32x2 b = ld8(mkrs(cnt + CNT(C_FILL)), 0u, 0u);
    if (lane == 0) __hip_atomic_store((gu32*)(cnt + CNT(C_POL)), b.y > b.x ? 3u : 2u, RLX_AGENT);
  }
}

__global__ __launch_bounds__(256, 1) void k_mega(Args a) {
  extern __shared__ __attribute__((aligned(16))) unsigned char shm[];
  volatile LAS int* sl = (volatile LAS int*)shm;
  unsigned ep = 0;
  if (threadIdx.x < 64u) ep = __hip_atomic_load((const gu32*)(a.ws + O_CNT) + W_EPOCH + (threadIdx.x == 0u ? 0u : 128u + (threadIdx.x & 7u) * 32u), RLX_AGENT);
  else {
    const char* pc; asm volatile("s_getpc_b64 %0" : "=s"(pc)); const unsigned o = ((blockIdx.x >> 3) * 48u + ((threadIdx.x - 64u) >> 2)) * 128u;
    touch(pc + (o < (unsigned)CPF ? o : 0u), lds_addr(shm) + 256u);
    if (threadIdx.x >= 128u && threadIdx.x < 192u) {
      const unsigned ln = threadIdx.x & 63u;
      touch(a.ws + (size_t)ln * (2 * MB) + 1572864, lds_addr(shm) + 512u); touch(a.ws + (size_t)(64u + (ln < 36u ? ln : 0u)) * (2 * MB) + 1572864, lds_addr(shm) + 512u);
      touch((const char*)a.out + (ln < 40u ? ln : 0u) * 131072u, lds_addr(shm) + 512u); } }
  if (threadIdx.x == 0) {
    const unsigned x0 = (unsigned)__builtin_amdgcn_s_getreg((3 << 11) | 20) & 7u;
    gu32* const cw = (gu32*)(a.ws + O_CNT);
    const unsigned first = x0 * 32u + ((blockIdx.x >> 3) & 31u);
    const unsigned tg = ep + 0x9e3779b1u;
    unsigned slot = first;
#pragma unroll 1
    for (unsigned i = 0; i < 256u; ++i) {
      slot = ((first + (i & ~31u)) & 224u) | ((first + i) & 31u);
      if (__hip_atomic_exchange(cw + W_SLOT + slot, tg, RLX_AGENT) != tg) break;
    }
    if (slot < 128u) {
      constexpr unsigned NS[4] = {LAY_S}, NA[4] = {LAY_A};
      const unsigned x = slot >> 5, k = slot & 31u;
      unsigned s0 = 0, a0 = 0, ns = NS[0], na = NA[0];
#pragma unroll
      for (unsigned i = 1; i < 4u; ++i) if (x >= i) { s0 += NS[i - 1]; a0 += NA[i - 1]; ns = NS[i]; na = NA[i]; }
      const unsigned iS = s0 + k, iA = a0 + k - ns, iR = slot - s0 - a0 - ns - na;
      slot = k < ns ? (iS >> 3) * 32u + (iS & 7u) : (k < ns + na ? (iA >> 2) * 32u + 28u + (iA & 3u) : (iR / 20u) * 32u + 8u + iR % 20u);
    }
    sl[0] = (int)slot; sl[1] = (int)tg;
  }
  __syncthreads();
  const int slot = __builtin_amdgcn_readfirstlane(sl[0]); const unsigned tag = (unsigned)__builtin_amdgcn_readfirstlane(sl[1]);
  VMW(0);
  __syncthreads();
  const int xq = (slot >> 5) & 3, kq = slot & 31, wave = __builtin_amdgcn_readfirstlane(threadIdx.x >> 6);
#define ENTER __builtin_amdgcn_s_waitcnt(0)
  { const int fq = kq - (slot < 128 ? 8 : 12); if (fq >= 0 && fq < 20) f_part(a, shm, (slot >> 7) * 16 + 4 * xq + fq / 5, fq % 5, wave, slot < 128, tag); }
  if (slot < 128) {
    if (kq < 8) {
      const int h = xq * 8 + kq;
      if (wave == 0) { ENTER; s_role<0>(a, shm, h, tag); } else if (wave == 1) { ENTER; s_role<1>(a, shm, h, tag); } else if (wave == 2) { ENTER; s_role<2>(a, shm, h, tag); } else { ENTER; s_role<3>(a, shm, h, tag); }
    } else if (kq < 28) {
      const int u = xq * 20 + kq - 8;
      ENTER; r_role(a, shm, u, wave);
      if (u == 0 && wave == 0 && lane_id() == 0) __hip_atomic_store((gu32*)(a.ws + O_CNT) + W_EPOCH, tag, RLX_AGENT);
    } else {
      ENTER;
      { unsigned d0 = 0, d1 = 0; const unsigned vo = (unsigned)(((kq - 28) * 4 + wave) * 8192 + lane_id() * 128);
        asm volatile("global_load_dword %0, %2, %3\n global_load_dword %1, %4, %5\n s_waitcnt vmcnt(0)" : "+v"(d0), "+v"(d1) : "v"(vo), "s"(a.Bx), "v"(vo < 10240u ? vo : 0u), "s"(a.mem0)); }
      calibrate(a.ws, a.mem0, (xq * 4 + kq - 28) * 4 + wave, tag);
      if (kq & 1) {
        const char* src = (const char*)a.Bx + ((xq * 2 + ((kq - 28) >> 1)) * 4 + wave) * 8192 + lane_id() * 128;
#pragma unroll 1
        for (int i = 0; i < APB; ++i) touch(src + i * 262144, lds_addr(shm));
        VMW(0);
      }
      att_role(a, xq * 8 + (kq - 28) * 2 + (wave & 1), wave >> 1); }
  } else {
    if (kq < 12) { ENTER;
      { unsigned d0 = 0; asm volatile("global_load_dword %0, %1, %2\n s_waitcnt vmcnt(0)" : "+v"(d0) : "v"((unsigned)((kq * 4 + wave) * 8192 + lane_id() * 128)), "s"(a.Bx)); }
      g_role<true>(a, shm, (kq < 6 ? 0 : 18) + xq * 6 + kq, wave, tag); }
    else { ENTER; g_role<false>(a, shm, xq * 20 + kq - 12, wave, tag); }
    { ENTER; int w2 = wave, s2 = slot; asm volatile("" : "+s"(w2), "+s"(s2)); a_role(a, shm, s2 & 15, (s2 >> 4) & 7, w2); }
  }
}

extern "C" void kernel_launch(void* const* d_in, const int* in_sizes, int n_in, void* d_out, int out_size, void* d_ws, size_t ws_size, hipStream_t stream) {
  Args a;
  a.Ax = (const u16*)d_in[0]; a.Awqkv = (const u16*)d_in[1]; a.Awo = (const u16*)d_in[2]; a.Bx = (const u16*)d_in[3]; a.mem0 = (const u16*)d_in[4];
  a.Wq = (const u16*)d_in[5]; a.Wk = (const u16*)d_in[6]; a.Wv = (const u16*)d_in[7]; a.Wo = (const u16*)d_in[8];
  a.out = (u16*)d_out;
  a.ws = (char*)(((size_t)d_ws + 2 * MB - 1) & ~(2 * MB - 1));
  if ((size_t)(a.ws - (char*)d_ws) + O_END > ws_size) return;
  const int dd[8] = {DDV};
  for (int i = 0; i < 8; ++i) a.d[i] = dd[i];
  static_assert(CNT(C_TOTAL) * 4 == 15 * 4096, "");
  (void)hipFuncSetAttribute((const void*)k_mega, hipFuncAttributeMaxDynamicSharedMemorySize, 163840);
  k_mega<<<256, 256, 163840, stream>>>(a);
}
```

```cpp
#include <hip/hip_runtime.h>
#include <hip/hip_bf16.h>
#include <stdint.h>

typedef unsigned short u16;
typedef unsigned long long u64;
typedef __attribute__((ext_vector_type(4))) float f32x4;
typedef __attribute__((address_space(1))) u64 gu64;
typedef __attribute__((address_space(1))) unsigned gu32;
typedef unsigned u32x8 __attribute__((ext_vector_type(8)));
typedef unsigned u32x4 __attribute__((ext_vector_type(4)));
typedef unsigned u32x2 __attribute__((ext_vector_type(2)));
typedef __attribute__((ext_vector_type(4))) short bf16x4;
typedef __attribute__((ext_vector_type(8))) __bf16 bfv8;
typedef __attribute__((ext_vector_type(16))) __bf16 bfv16;
typedef __bf16 bf16x2_t __attribute__((ext_vector_type(2)));
typedef float f32x2_t __attribute__((ext_vector_type(2)));
typedef unsigned v2u_t __attribute__((ext_vector_type(2)));
#define RLX_AGENT __ATOMIC_RELAXED, __HIP_MEMORY_SCOPE_AGENT
#define LAS __attribute__((address_space(3)))

__device__ __forceinline__ float bf2f(u16 v) { return __uint_as_float(((unsigned)v) << 16); }
__device__ __forceinline__ u16 f2bf(float f) { return __builtin_bit_cast(u16, (__bf16)f); }
__device__ __forceinline__ float rbf(float f) { return bf2f(f2bf(f)); }
__device__ __forceinline__ unsigned cvtpk(float lo, float hi) { f32x2_t v = {lo, hi}; return __builtin_bit_cast(unsigned, __builtin_convertvector(v, bf16x2_t)); }
__device__ __forceinline__ __amdgpu_buffer_rsrc_t mkrs(const void* p) { return __builtin_amdgcn_make_buffer_rsrc((void*)p, 0, 0x7fffffff, 0x00020000); }
__device__ __forceinline__ u32x4 ld16(__amdgpu_buffer_rsrc_t rs, unsigned voff, unsigned soff) { return __builtin_amdgcn_raw_buffer_load_b128(rs, (int)voff, (int)soff, 16); }
__device__ __forceinline__ u32x2 ld8(__amdgpu_buffer_rsrc_t rs, unsigned voff, unsigned soff) { return __builtin_amdgcn_raw_buffer_load_b64(rs, (int)voff, (int)soff, 16); }
__device__ __forceinline__ void st16(__amdgpu_buffer_rsrc_t rs, unsigned voff, unsigned soff, u32x4 w) { __builtin_amdgcn_raw_buffer_store_b128(w, rs, (int)voff, (int)soff, 16); }
__device__ __forceinline__ void st8(__amdgpu_buffer_rsrc_t rs, unsigned voff, unsigned soff, unsigned lo, unsigned hi) { u32x2 w = {lo, hi}; __builtin_amdgcn_raw_buffer_store_b64(w, rs, (int)voff, (int)soff, 16); }
__device__ __forceinline__ float max16(float x) { v2u_t r = __builtin_amdgcn_permlane16_swap(__float_as_uint(x), __float_as_uint(x), false, false); return fmaxf(__uint_as_float(r[0]), __uint_as_float(r[1])); }
__device__ __forceinline__ float max32(float x) { v2u_t r = __builtin_amdgcn_permlane32_swap(__float_as_uint(x), __float_as_uint(x), false, false); return fmaxf(__uint_as_float(r[0]), __uint_as_float(r[1])); }
__device__ __forceinline__ float sum16(float x) { v2u_t r = __builtin_amdgcn_permlane16_swap(__float_as_uint(x), __float_as_uint(x), false, false); return __uint_as_float(r[0]) + __uint_as_float(r[1]); }
__device__ __forceinline__ float sum32(float x) { v2u_t r = __builtin_amdgcn_permlane32_swap(__float_as_uint(x), __float_as_uint(x), false, false); return __uint_as_float(r[0]) + __uint_as_float(r[1]); }
__device__ __forceinline__ float tred32(float a, float b) { v2u_t r = __builtin_amdgcn_permlane32_swap(__float_as_uint(a), __float_as_uint(b), false, false); return __uint_as_float(r[0]) + __uint_as_float(r[1]); }
__device__ __forceinline__ unsigned half_of(const char* base, unsigned off, unsigned side, unsigned pol) {
  return off + ((((unsigned)(((size_t)base + off) >> 21) ^ side ^ pol) & 1u) << 13);
}
__device__ __forceinline__ unsigned side_map(const void* base, unsigned L, unsigned side, unsigned pol) { return half_of((const char*)base, (L >> 13) << 14, side, pol) + (L & 8191u); }
constexpr unsigned CH_SPIN_CAP = 1u << 18;
__device__ __forceinline__ bool spin_check(unsigned& spins, unsigned* tmop) {
  asm volatile("" ::: "memory");
  ++spins;
  if ((spins & 63u) == 0u) {
    gu32* tmo = (gu32*)tmop;
    if (__builtin_amdgcn_readfirstlane(__hip_atomic_load(tmo, RLX_AGENT))) return false;
    if (spins > CH_SPIN_CAP) { __hip_atomic_store(tmo, 1u, RLX_AGENT); return false; }
  }
  return true;
}
__device__ __forceinline__ unsigned xcc_side() { return ((unsigned)__builtin_amdgcn_s_getreg((3 << 11) | 20) >> 2) & 1u; }
__device__ __forceinline__ f32x4 mfma(u32x4 a, u32x4 b, f32x4 c) { return __builtin_amdgcn_mfma_f32_16x16x32_bf16(__builtin_bit_cast(bfv8, a), __builtin_bit_cast(bfv8, b), c, 0, 0, 0); }
__device__ __forceinline__ f32x4 smf(u32x4 a, u32x8 b, f32x4 c, int ix) { return __builtin_amdgcn_smfmac_f32_16x16x64_bf16(__builtin_bit_cast(bfv8, a), __builtin_bit_cast(bfv16, b), c, ix, 0, 0); }
__device__ __forceinline__ u32x4 lo4(u32x8 v) { return __builtin_shufflevector(v, v, 0, 1, 2, 3); }
__device__ __forceinline__ u32x4 hi4(u32x8 v) { return __builtin_shufflevector(v, v, 4, 5, 6, 7); }
__device__ __forceinline__ u32x8 cat8(u32x4 a, u32x4 b) { return __builtin_shufflevector(a, b, 0, 1, 2, 3, 4, 5, 6, 7); }
#define BAR() do { asm volatile("s_waitcnt lgkmcnt(0)" ::: "memory"); __builtin_amdgcn_s_barrier(); asm volatile("" ::: "memory"); } while (0)
#define SB() __builtin_amdgcn_sched_barrier(0)
#define VMW(n) asm volatile("s_waitcnt vmcnt(" #n ")" ::: "memory")
#define ZERO4 ((f32x4){0.f, 0.f, 0.f, 0.f})
__device__ __forceinline__ u32x2 trrd(const unsigned char* lds, unsigned off) { return __builtin_bit_cast(u32x2, __builtin_amdgcn_ds_read_tr16_b64_v4i16((LAS bf16x4*)(lds + off))); }
__device__ __forceinline__ u32x4 trrd2(const unsigned char* lds, unsigned off, unsigned d) { const u32x2 lo = trrd(lds, off), hi = trrd(lds, off + d); return (u32x4){lo.x, lo.y, hi.x, hi.y}; }

#define TSK 8
#define APN 2
#define APU 1
#define GFW 6
#define APB 10
#define OPH 3
#define RSELF 3
#define RN3 20
#define XSL 0
#define NU1 4
#define AQ0 2
#define AU0 3
#define LAY_S 16, 0, 16, 0
#define LAY_A 8, 0, 8, 0
#define DUR0 132u
#define BKO 2
#define RFW 4
#define CPF 116000
#define FD0 6
#define CALN 5
#define RLAG 4
#define RN1 7
#define RN2 9
#define DDV 68, 74, 8, 8, 0, 2, 1, 88
#define FSL 4
struct Args {
  const u16* mem0; const u16* Wq; const u16* Wk; const u16* Wv; const u16* Wo; const u16* Bx; const u16* Ax; const u16* Awqkv; const u16* Awo;
  u16* out; char* ws;
  int d[8];
};
constexpr size_t MB = 1u << 20;
constexpr size_t O_CNT = 1 * MB, O_CAL = O_CNT + 65536, O_X1 = 2 * MB;
constexpr unsigned X1_STEP = 32768;
constexpr size_t O_FF = 4 * MB, O_KF = O_FF + 25 * MB, O_QF = O_KF + 24 * MB, O_KM = O_QF + 24 * MB, O_PT = O_KM + 7 * MB, O_UO = O_PT + 8 * MB,
                 O_PS = O_UO + 96 * MB, O_OA = O_PS + 8 * MB, O_END = O_OA + 3 * MB;
static_assert(O_X1 + 64 * 32768 <= O_FF, "");
#define PT_BLK(g_, c_) ((((g_) * ((g_) + 3) / 2 + ((c_) == 4 ? 0 : (c_) + 1)) >> 1) * 16384 + (((g_) * ((g_) + 3) / 2 + ((c_) == 4 ? 0 : (c_) + 1)) & 1) * 4096)
constexpr unsigned PT_SEG = 131072;
constexpr unsigned QK_H = 64 * 12288, QK_S = 12288, U_U = 64 * 16384, U_S = 16384, KM_H = 64 * 3072, KM_S = 3072;
#define CNT(i) (32 * (i))
constexpr int PRE = 8;
#define PSLOT(s_) (((s_) >> 5) * (4 << 20) + ((s_) & 31) * 65536)
constexpr int C_TMO = 8, C_POL = 9, C_FILL = 10, C_AO = 16, C_PT_ = 32, C_PS = 64, C_PO = 96, C_TOK = 192, C_FF = 448, C_TOTAL = 480;
__device__ __forceinline__ float nopk(float x) { asm("" : "+v"(x)); return x; }
__device__ __forceinline__ float fmx(float a, float b) { float big = __builtin_inff(); asm("" : "+s"(big)); return __builtin_amdgcn_fmed3f(a, b, big); }
__device__ __forceinline__ float t32(float a, float b) { return nopk(tred32(a, b)); }
__device__ __forceinline__ int lane_id() { int l = (int)__builtin_amdgcn_mbcnt_hi(~0u, __builtin_amdgcn_mbcnt_lo(~0u, 0u)); asm volatile("" : "+v"(l)); return l; }
#define RT_MARK(t_) asm volatile("s_memrealtime %0\n s_waitcnt lgkmcnt(0)" : "=s"(t_) :: "memory")
__device__ __forceinline__ unsigned wait_rt(unsigned target) {
  int r;
#define RT_S "s_memrealtime vcc\n s_waitcnt lgkmcnt(0)\n s_sub_i32 %0, %1, vcc_lo\n"
#define RT_F RT_S "s_cmp_le_i32 %0, 4\n s_cbranch_scc1 2f\n"
  asm volatile("0: " RT_S "s_cmp_gt_i32 %0, 400\n s_cbranch_scc1 3f\n s_cmp_le_i32 %0, 13\n s_cbranch_scc1 1f\n s_sleep 1\n s_branch 0b\n"
               "1: " RT_F RT_F RT_F RT_F RT_F "s_branch 1b\n"
               "2: s_cmp_lt_i32 %0, 2\n s_cbranch_scc1 3f\n s_nop 2\n s_cmp_lt_i32 %0, 3\n s_cbranch_scc1 3f\n s_nop 2\n s_cmp_lt_i32 %0, 4\n s_cbranch_scc1 3f\n s_nop 6\n 3:"
               : "=&s"(r) : "s"(__builtin_amdgcn_readfirstlane(target)) : "vcc", "scc", "memory");
  return (unsigned)r - 1u < 4u ? target : target - (unsigned)r;
}
__device__ __forceinline__ unsigned ts_max(unsigned v) {
  if (__ballot(v == ~0u)) return ~0u;
  const unsigned base = (unsigned)__builtin_amdgcn_readfirstlane(v) - 4096u;
  unsigned d = v - base;
  d = max(d, (unsigned)__builtin_amdgcn_mov_dpp((int)d, 0x121, 0xf, 0xf, true)); d = max(d, (unsigned)__builtin_amdgcn_mov_dpp((int)d, 0x122, 0xf, 0xf, true));
  d = max(d, (unsigned)__builtin_amdgcn_mov_dpp((int)d, 0x124, 0xf, 0xf, true)); d = max(d, (unsigned)__builtin_amdgcn_mov_dpp((int)d, 0x128, 0xf, 0xf, true));
  return (base + max((unsigned)__builtin_amdgcn_readlane(d, 0), (unsigned)__builtin_amdgcn_readlane(d, 16)));
}
constexpr int TS_OFF = 32768 + 4096;
__device__ __forceinline__ unsigned ts_wait(__amdgpu_buffer_rsrc_t rs, int so) {
  unsigned v = ~0u;
  for (int n = 0; n < 64 && v == ~0u; ++n) { __builtin_amdgcn_s_sleep(2); asm volatile("" ::: "memory"); v = ts_max(__builtin_amdgcn_raw_buffer_load_b32(rs, (lane_id() & 31) * 128, so, 16)); }
  return v;
}
__device__ __forceinline__ int adapt(int d, unsigned failed, int base, int s, int up, int mask) {
  int r;
  asm volatile("s_add_i32 %0, %1, -1\n s_max_i32 %0, %0, %3\n s_and_b32 vcc_lo, %4, %6\n s_cselect_b32 %0, %1, %0\n s_add_i32 vcc_lo, %1, %5\n s_cmp_lg_u32 %2, 0\n s_cselect_b32 %0, vcc_lo, %0\n s_cmp_gt_i32 %4, 3\n s_cselect_b32 %0, %0, %1"
               : "=&s"(r) : "s"(__builtin_amdgcn_readfirstlane(d)), "s"(__builtin_amdgcn_readfirstlane(failed)), "s"(base), "s"(s), "s"(up), "s"(mask) : "vcc", "scc");
  return r;
}
#define ADAPT(d_, failed_, base_) d_ = adapt(d_, failed_, base_, s, a.d[5], a.d[6])
#define PINV(x) asm volatile("" : "+v"(x))
constexpr int L_MEM = 0, L_QI = 10240;
constexpr int L_B0 = 16384, L_B1 = 65536, L_B2 = 114688;
constexpr int L_KX = L_B1, L_FLG = 14080, L_ST = 14336, L_KI = L_B1 + 28672;

#define AFR(k_) (*(const u32x4*)(lds + afb + (k_) * 1024))

#define TR8_ASM "ds_read_b64_tr_b16 %0, %8\n ds_read_b64_tr_b16 %1, %8 offset:1536\n ds_read_b64_tr_b16 %2, %8 offset:12288\n ds_read_b64_tr_b16 %3, %8 offset:13824\n" \
               "ds_read_b64_tr_b16 %4, %8 offset:24576\n ds_read_b64_tr_b16 %5, %8 offset:26112\n ds_read_b64_tr_b16 %6, %8 offset:36864\n ds_read_b64_tr_b16 %7, %8 offset:38400\n s_waitcnt lgkmcnt(0)"
__device__ __forceinline__ void tr8(const unsigned ad, u32x8& f0, u32x8& f1) {
  u32x2 a, b, c, d, e, f, g, h;
  asm volatile(TR8_ASM : "=&v"(a), "=&v"(b), "=&v"(c), "=&v"(d), "=&v"(e), "=&v"(f), "=&v"(g), "=&v"(h) : "v"(ad) : "memory");
  f0 = (u32x8){a.x, a.y, b.x, b.y, c.x, c.y, d.x, d.y}; f1 = (u32x8){e.x, e.y, f.x, f.y, g.x, g.y, h.x, h.y};
}
__device__ __forceinline__ void glds(const void* sbase, unsigned voff, unsigned ldsdst) {
  unsigned keep;
  asm volatile("s_mov_b32 %0, m0\n s_mov_b32 m0, %3\n s_nop 0\n global_load_lds_dwordx4 %1, %2\n s_mov_b32 m0, %0" : "=&s"(keep) : "v"(voff), "s"(sbase), "s"(__builtin_amdgcn_readfirstlane(ldsdst)) : "memory");
}
__device__ __forceinline__ void touch(const void* p, unsigned ldsdump) {
  unsigned keep;
  asm volatile("s_mov_b32 %0, m0\n s_mov_b32 m0, %2\n s_nop 0\n global_load_lds_dword %1, off\n s_mov_b32 m0, %0" : "=&s"(keep) : "v"(p), "s"(__builtin_amdgcn_readfirstlane(ldsdump)) : "memory");
}
__device__ __forceinline__ unsigned lds_addr(const unsigned char* p) { return (unsigned)(size_t)(const LAS unsigned char*)p; }
__device__ __forceinline__ unsigned ld_word(const unsigned* p) { return (unsigned)__builtin_amdgcn_readfirstlane(__hip_atomic_load((const gu32*)p, RLX_AGENT)); }
template <int SLEEP>
__device__ __forceinline__ unsigned wait_ge(const unsigned* p, unsigned target, unsigned* tmo) {
  unsigned spins = 0, v;
  while ((v = ld_word(p)) < target) { __builtin_amdgcn_s_sleep(SLEEP); if (!spin_check(spins, tmo)) break; }
  asm volatile("" ::: "memory");
  return v;
}
constexpr int W_DONE = CNT(C_TOTAL), W_EPOCH = W_DONE + 128, W_SLOT = W_DONE + 256;
__device__ __forceinline__ void init_wait(const char* ws, unsigned tag) {
  const gu32* p = (const gu32*)(ws + O_CNT) + W_DONE + lane_id();
  for (unsigned n = 0; n < CH_SPIN_CAP && !__all(__hip_atomic_load(p, RLX_AGENT) == tag); ++n) __builtin_amdgcn_s_sleep(8);
  asm volatile("" ::: "memory");
}
template <int SLEEP>
__device__ __forceinline__ unsigned wait_t(const unsigned* p, const bool both, unsigned target, unsigned* tmo) {
  unsigned spins = 0, v;
  for (;;) {
    const u32x4 w = ld16(mkrs(p), 0u, 0u);
    v = min(min(w.x, w.y), w.z);
    if (both) { const u32x4 w2 = ld16(mkrs(p), 16u, 0u); v = min(v, min(min(w2.x, w2.y), w2.z)); }
    v = (unsigned)__builtin_amdgcn_readfirstlane(v);
    if (v >= target) break;
    __builtin_amdgcn_s_sleep(SLEEP); if (!spin_check(spins, tmo)) break;
  }
  asm volatile("" ::: "memory");
  return v;
}
__device__ __forceinline__ unsigned wait_u(const unsigned* p, unsigned target, unsigned* tmo) {
  unsigned spins = 0, v;
  for (;;) {
    const u32x4 w = ld16(mkrs(p), 0u, 0u);
    v = (unsigned)__builtin_amdgcn_readfirstlane(min(min(w.x, w.y), min(w.z, w.w)));
    if (v >= target) break;
    __builtin_amdgcn_s_sleep(4); if (!spin_check(spins, tmo)) break;
  }
  asm volatile("" ::: "memory");
  return v;
}
__device__ __forceinline__ unsigned min3u(unsigned a, unsigned b, unsigned c) { return min(min(a, b), c); }
template <int W>
__device__ __forceinline__ void s_role(const Args& a, unsigned char* lds, const int h, const unsigned tag) {
  char* ws = a.ws; asm volatile("" : "+s"(ws));
  unsigned* const cnt = (unsigned*)(ws + O_CNT); unsigned* const tmo = cnt + CNT(C_TMO);
  char* const X1 = ws + O_X1; char* const P = ws + O_PS;
  const int lane = lane_id(), l15 = lane & 15, lq = lane >> 4;
  constexpr int wave = W;
#define WG00 "{a[0:7]}"
#define WG01 "{a[8:15]}"
#define WG02 "{a[16:23]}"
#define WG03 "{a[24:31]}"
#define WG04 "{a[32:39]}"
#define WG05 "{a[40:47]}"
#define WG06 "{a[48:55]}"
#define WG07 "{a[56:63]}"
#define WG10 "{a[64:71]}"
#define WG11 "{a[72:79]}"
#define WG12 "{a[80:87]}"
#define WG13 "{a[88:95]}"
#define WG14 "{a[96:103]}"
#define WG15 "{a[104:111]}"
#define WG16 "{a[112:119]}"
#define WG17 "{a[120:127]}"
#define WG20 "{a[128:135]}"
#define WG21 "{a[136:143]}"
#define WG22 "{a[144:151]}"
#define WG23 "{a[152:159]}"
#define WG24 "{a[160:167]}"
#define WG25 "{a[168:175]}"
#define WG26 "{a[176:183]}"
#define WG27 "{a[184:191]}"
#define WG30 "{a[192:199]}"
#define WG31 "{a[200:207]}"
#define WG32 "{a[208:215]}"
#define WG33 "{a[216:223]}"
#define WG34 "{a[224:231]}"
#define WG35 "{a[232:239]}"
#define WG36 "{v[88:95]}"
#define WG37 "{v[96:103]}"
#define WG40 "{v[104:111]}"
#define WG41 "{v[112:119]}"
#define WG42 "{v[120:127]}"
#define WG43 "{v[128:135]}"
#define WG44 "{v[136:143]}"
#define WG45 "{v[144:151]}"
#define WG46 "{v[152:159]}"
#define WG47 "{v[160:167]}"
#define WG50 "{v[168:175]}"
#define WG51 "{v[176:183]}"
#define WG52 "{v[184:191]}"
#define WG53 "{v[192:199]}"
#define WG54 "{v[200:207]}"
#define WG55 "{v[208:215]}"
#define WG56 "{v[216:223]}"
#define WG57 "{v[224:231]}"
#define TRR(c, a, b, o) "ds_read_b64_tr_b16 " #c "[" #a ":" #b "], %2 offset:" #o "\n"
#define TW00 TRR(a, 0, 1, 0) TRR(a, 2, 3, 1536) TRR(a, 4, 5, 12288) TRR(a, 6, 7, 13824) TRR(a, 8, 9, 24576) TRR(a, 10, 11, 26112) TRR(a, 12, 13, 36864) TRR(a, 14, 15, 38400)
#define TW01 TRR(a, 16, 17, 0) TRR(a, 18, 19, 1536) TRR(a, 20, 21, 12288) TRR(a, 22, 23, 13824) TRR(a, 24, 25, 24576) TRR(a, 26, 27, 26112) TRR(a, 28, 29, 36864) TRR(a, 30, 31, 38400)
#define TW02 TRR(a, 32, 33, 0) TRR(a, 34, 35, 1536) TRR(a, 36, 37, 12288) TRR(a, 38, 39, 13824) TRR(a, 40, 41, 24576) TRR(a, 42, 43, 26112) TRR(a, 44, 45, 36864) TRR(a, 46, 47, 38400)
#define TW03 TRR(a, 48, 49, 0) TRR(a, 50, 51, 1536) TRR(a, 52, 53, 12288) TRR(a, 54, 55, 13824) TRR(a, 56, 57, 24576) TRR(a, 58, 59, 26112) TRR(a, 60, 61, 36864) TRR(a, 62, 63, 38400)
#define TW10 TRR(a, 64, 65, 0) TRR(a, 66, 67, 1536) TRR(a, 68, 69, 12288) TRR(a, 70, 71, 13824) TRR(a, 72, 73, 24576) TRR(a, 74, 75, 26112) TRR(a, 76, 77, 36864) TRR(a, 78, 79, 38400)
#define TW11 TRR(a, 80, 81, 0) TRR(a, 82, 83, 1536) TRR(a, 84, 85, 12288) TRR(a, 86, 87, 13824) TRR(a, 88, 89, 24576) TRR(a, 90, 91, 26112) TRR(a, 92, 93, 36864) TRR(a, 94, 95, 38400)
#define TW12 TRR(a, 96, 97, 0) TRR(a, 98, 99, 1536) TRR(a, 100, 101, 12288) TRR(a, 102, 103, 13824) TRR(a, 104, 105, 24576) TRR(a, 106, 107, 26112) TRR(a, 108, 109, 36864) TRR(a, 110, 111, 38400)
#define TW13 TRR(a, 112, 113, 0) TRR(a, 114, 115, 1536) TRR(a, 116, 117, 12288) TRR(a, 118, 119, 13824) TRR(a, 120, 121, 24576) TRR(a, 122, 123, 26112) TRR(a, 124, 125, 36864) TRR(a, 126, 127, 38400)
#define TW20 TRR(a, 128, 129, 0) TRR(a, 130, 131, 1536) TRR(a, 132, 133, 12288) TRR(a, 134, 135, 13824) TRR(a, 136, 137, 24576) TRR(a, 138, 139, 26112) TRR(a, 140, 141, 36864) TRR(a, 142, 143, 38400)
#define TW21 TRR(a, 144, 145, 0) TRR(a, 146, 147, 1536) TRR(a, 148, 149, 12288) TRR(a, 150, 151, 13824) TRR(a, 152, 153, 24576) TRR(a, 154, 155, 26112) TRR(a, 156, 157, 36864) TRR(a, 158, 159, 38400)
#define TW22 TRR(a, 160, 161, 0) TRR(a, 162, 163, 1536) TRR(a, 164, 165, 12288) TRR(a, 166, 167, 13824) TRR(a, 168, 169, 24576) TRR(a, 170, 171, 26112) TRR(a, 172, 173, 36864) TRR(a, 174, 175, 38400)
#define TW23 TRR(a, 176, 177, 0) TRR(a, 178, 179, 1536) TRR(a, 180, 181, 12288) TRR(a, 182, 183, 13824) TRR(a, 184, 185, 24576) TRR(a, 186, 187, 26112) TRR(a, 188, 189, 36864) TRR(a, 190, 191, 38400)
#define TW30 TRR(a, 192, 193, 0) TRR(a, 194, 195, 1536) TRR(a, 196, 197, 12288) TRR(a, 198, 199, 13824) TRR(a, 200, 201, 24576) TRR(a, 202, 203, 26112) TRR(a, 204, 205, 36864) TRR(a, 206, 207, 38400)
#define TW31 TRR(a, 208, 209, 0) TRR(a, 210, 211, 1536) TRR(a, 212, 213, 12288) TRR(a, 214, 215, 13824) TRR(a, 216, 217, 24576) TRR(a, 218, 219, 26112) TRR(a, 220, 221, 36864) TRR(a, 222, 223, 38400)
#define TW32 TRR(a, 224, 225, 0) TRR(a, 226, 227, 1536) TRR(a, 228, 229, 12288) TRR(a, 230, 231, 13824) TRR(a, 232, 233, 24576) TRR(a, 234, 235, 26112) TRR(a, 236, 237, 36864) TRR(a, 238, 239, 38400)
#define TW33 TRR(v, 88, 89, 0) TRR(v, 90, 91, 1536) TRR(v, 92, 93, 12288) TRR(v, 94, 95, 13824) TRR(v, 96, 97, 24576) TRR(v, 98, 99, 26112) TRR(v, 100, 101, 36864) TRR(v, 102, 103, 38400)
#define TW40 TRR(v, 104, 105, 0) TRR(v, 106, 107, 1536) TRR(v, 108, 109, 12288) TRR(v, 110, 111, 13824) TRR(v, 112, 113, 24576) TRR(v, 114, 115, 26112) TRR(v, 116, 117, 36864) TRR(v, 118, 119, 38400)
#define TW41 TRR(v, 120, 121, 0) TRR(v, 122, 123, 1536) TRR(v, 124, 125, 12288) TRR(v, 126, 127, 13824) TRR(v, 128, 129, 24576) TRR(v, 130, 131, 26112) TRR(v, 132, 133, 36864) TRR(v, 134, 135, 38400)
#define TW42 TRR(v, 136, 137, 0) TRR(v, 138, 139, 1536) TRR(v, 140, 141, 12288) TRR(v, 142, 143, 13824) TRR(v, 144, 145, 24576) TRR(v, 146, 147, 26112) TRR(v, 148, 149, 36864) TRR(v, 150, 151, 38400)
#define TW43 TRR(v, 152, 153, 0) TRR(v, 154, 155, 1536) TRR(v, 156, 157, 12288) TRR(v, 158, 159, 13824) TRR(v, 160, 161, 24576) TRR(v, 162, 163, 26112) TRR(v, 164, 165, 36864) TRR(v, 166, 167, 38400)
#define TW50 TRR(v, 168, 169, 0) TRR(v, 170, 171, 1536) TRR(v, 172, 173, 12288) TRR(v, 174, 175, 13824) TRR(v, 176, 177, 24576) TRR(v, 178, 179, 26112) TRR(v, 180, 181, 36864) TRR(v, 182, 183, 38400)
#define TW51 TRR(v, 184, 185, 0) TRR(v, 186, 187, 1536) TRR(v, 188, 189, 12288) TRR(v, 190, 191, 13824) TRR(v, 192, 193, 24576) TRR(v, 194, 195, 26112) TRR(v, 196, 197, 36864) TRR(v, 198, 199, 38400)
#define TW52 TRR(v, 200, 201, 0) TRR(v, 202, 203, 1536) TRR(v, 204, 205, 12288) TRR(v, 206, 207, 13824) TRR(v, 208, 209, 24576) TRR(v, 210, 211, 26112) TRR(v, 212, 213, 36864) TRR(v, 214, 215, 38400)
#define TW53 TRR(v, 216, 217, 0) TRR(v, 218, 219, 1536) TRR(v, 220, 221, 12288) TRR(v, 222, 223, 13824) TRR(v, 224, 225, 24576) TRR(v, 226, 227, 26112) TRR(v, 228, 229, 36864) TRR(v, 230, 231, 38400)
#define TWL(n, j, k0, k1, ad_) asm volatile(TW##n##j "s_waitcnt lgkmcnt(0)" : "=&" WG##n##k0(wt[n][k0]), "=&" WG##n##k1(wt[n][k1]) : "v"(ad_) : "memory")
#define TWN(n, j_, ad_) do { if ((j_) == 0) TWL(n, 0, 0, 1, ad_); else if ((j_) == 1) TWL(n, 1, 2, 3, ad_); else if ((j_) == 2) TWL(n, 2, 4, 5, ad_); else TWL(n, 3, 6, 7, ad_); } while (0)
  u32x8 wt[6][8];
  unsigned tro[3];
  {
    const unsigned row = (unsigned)(8 * lq + (l15 >> 2)), swz = ((row >> 1) & 1u) * 2u + ((row >> 3) & 1u) * 4u;
#pragma unroll
    for (int t = 0; t < 3; ++t) tro[t] = row * 384u + (((unsigned)(2 * (4 * t + wave) + ((l15 & 3) >> 1))) ^ swz) * 16u + (unsigned)(l15 & 1) * 8u;
  }
#define SBUF(i_) (((i_) % 3) == 0 ? L_B0 : (((i_) % 3) == 1 ? L_B1 : L_B2))
  unsigned svo[12]; const int sq0 = 3 * ((h >> 2) & 3);
#pragma unroll
  for (int q_ = 0; q_ < 12; ++q_) { const int qr_ = q_ + sq0, ins = wave + 4 * (qr_ < 12 ? qr_ : qr_ - 12); const unsigned c = (unsigned)(ins * 64 + lane), r = c / 24u, pc = c - r * 24u, sw = ((r >> 1) & 1u) * 2u + ((r >> 3) & 1u) * 4u;
    svo[q_] = ((r & ~10u) | ((r & 2u) << 2) | ((r & 8u) >> 2)) * 12288u + 16u * (pc ^ sw); PINV(svo[q_]); }
#define SDMA(i_) do { const char* W_ = (const char*)((((i_) & 1) ? a.Wk : a.Wq) + (size_t)(128 * (((i_) >> 1) < 4 ? ((((i_) >> 1) + h) & 3) : 4)) * 6144 + 192 * h);        \
    _Pragma("unroll") for (int q_ = 0; q_ < 12; ++q_) { const int qr_ = q_ + sq0; glds(W_, svo[q_], lds_addr(lds) + (unsigned)(SBUF(i_) + (wave + 4 * (qr_ < 12 ? qr_ : qr_ - 12)) * 1024)); } SB(); } while (0)
  SDMA(0); SDMA(1); SDMA(2);
#pragma unroll
  for (int i = 0; i < 10; ++i) {
    if (i < 8) VMW(24); else if (i == 8) VMW(12); else { init_wait(ws, tag); VMW(0); }
    BAR();
    u32x8 res[3][2];
    const unsigned tb = lds_addr(lds) + (unsigned)SBUF(i); const int sl = ((i >> 1) - W) & 3;
    if (i >= 8) {
#pragma unroll
      for (int t = 0; t < 3; ++t) tr8(tb + tro[t], res[t][0], res[t][1]);
    } else if (i & 1) { TWN(3, sl, tb + tro[0]); TWN(4, sl, tb + tro[1]); TWN(5, sl, tb + tro[2]); }
    else { TWN(0, sl, tb + tro[0]); TWN(1, sl, tb + tro[1]); TWN(2, sl, tb + tro[2]); }
    BAR();
    if (i + 3 < 10) SDMA(i + 3);
    if (i >= 8) {
#pragma unroll
      for (int t = 0; t < 3; ++t)
#pragma unroll
        for (int k = 0; k < 2; ++k) {
          unsigned char* dst = lds + SBUF(i) + wave * 12288 + t * 4096 + k * 2048 + lane * 16;
          *(u32x4*)dst = lo4(res[t][k]); *(u32x4*)(dst + 1024) = hi4(res[t][k]);
        }
    }
  }
  const unsigned pol = wait_ge<8>(cnt + CNT(C_POL), 1u, tmo) & 1u;
  const __amdgpu_buffer_rsrc_t rs_p = mkrs(P), rs_kf = mkrs(ws + O_KF + (size_t)h * QK_H), rs_tp = mkrs(cnt + CNT(C_PT_ + h));
  unsigned tprog = 0;
  constexpr int kxs = 6 * W;
  if (W < 2) {
    tprog = wait_t<4>(cnt + CNT(C_PT_ + h), false, 1u, tmo);
#pragma unroll
    for (int i = 0; i < 6; ++i) *(u32x4*)(lds + L_KX + (kxs + i) * 1024 + lane * 16) = ld16(rs_kf, (unsigned)((kxs + i) * 1024 + lane * 16), 0u);
  } else if (W == 2) { if (lane < 32) *(unsigned*)(lds + L_FLG + 4 * lane) = lane == 24 ? DUR0 + 8u : 0u; *(u32x4*)(lds + L_ST + lane * 16) = (u32x4){0u, 0u, 0u, 0u}; }
  const unsigned lb = lds_addr(lds);
  unsigned afb = lb + (unsigned)(lane * 16), trq = (unsigned)(L_B2 + wave * 12288) + afb; PINV(trq);
#define trk (afb + (unsigned)(L_B0 + wave * 12288))
#define LP(T_, ad_) ((LAS T_*)(ad_))
  const __amdgpu_buffer_rsrc_t rs_x1 = mkrs(X1 - lb), rs_kn = mkrs(ws + O_KF + (size_t)h * QK_H + kxs * 1024 - lb);
  int ix = l15 < 8 ? 0x4444 : 0xEEEE; PINV(ix); int ix2 = (l15 & 2) ? 0xEEEE : 0x4444;
  const unsigned cp = (unsigned)(2 * (lq & 1) + (lq >> 1));
  unsigned qwa = lb + (unsigned)(L_QI + wave * 320 + ((l15 >> 2) * 2 + (l15 & 1)) * 32 + ((l15 >> 1) & 1) * 4) + cp * 8u, kwa = lb + (unsigned)(L_KI + (16 * wave + l15) * 16) + cp * 4u;
  unsigned qra = lb + (unsigned)(L_QI + (2 * (lq >> 1) + (l15 >> 3)) * 320 + (lq & 1) * 128 + ((l15 >> 2) & 1) * 32 + (l15 & 3) * 8);
  unsigned kra = lb + (unsigned)(L_KI + (4 * lq + (l15 >> 2)) * 16 + (l15 & 1) * 8);
  unsigned lwa = lb + (unsigned)(L_ST + 2 * lq * 128 + (16 * wave + l15) * 4), fl0 = lb + (unsigned)L_FLG;
  PINV(afb); PINV(qwa); PINV(kwa); PINV(fl0); if (W < 3) { PINV(qra); PINV(ix2); } if (W >= 2) PINV(kra); if (W < 2) PINV(lwa);
  unsigned kxb = (unsigned)(L_KX + wave * 6144) + afb; if (W < 2) PINV(kxb);
  const bool hi8 = l15 >= 8;
  const __amdgpu_buffer_rsrc_t rs_ts = mkrs(P + half_of(P, 0u, 0u, pol) + TS_OFF);
  const __amdgpu_buffer_rsrc_t rs_ps = mkrs(cnt + CNT(C_PS));
  int tso = lane == 0 ? 128 * h : (int)0x80000000u; if (W == 3) PINV(tso);
  unsigned po = side_map(P, (unsigned)((4 * h + ((lane & 7) >> 1)) * 128 + (lane >> 3) * 16 + (lane & 1) * 8), 0u, pol);
  unsigned pm = (lane & 1) == 0 ? side_map(P, (unsigned)(16384 + h * 128 + lane * 2), 0u, pol) : 0x80000000u;
  float lsc = 1.4426950408889634f * 0.07216878364870323f;
  if (W == 2) { PINV(po); PINV(pm); asm volatile("" : "+s"(lsc)); }
  const unsigned xh = half_of(X1, 0u, 0u, pol), gb = (unsigned)((W + h) & 3) * 2048u;
  unsigned lg0 = 0, tb2 = 0, dur = DUR0, tsv = ~0u; int dly = a.d[0] + 2 * a.d[4]; asm volatile("" : "+s"(dly));
  BAR();
  constexpr int P0 = 2 * W, P1 = 2 * W + 1, R2 = W >> 1;
#define OT(i_) ((2 * W + 2 + (i_)) & 7)
#define AR0 "v[232:235]"
#define AR1 "v[236:239]"
#define AR2 "v[240:243]"
#define AR3 "v[244:247]"
#define AR4 "v[248:251]"
#define AR5 "v[252:255]"
#define SW01 "v_permlane32_swap_b32 v232, v234\n"
#define SW02 "v_permlane32_swap_b32 v233, v235\n"
#define SW11 "v_permlane32_swap_b32 v236, v238\n"
#define SW12 "v_permlane32_swap_b32 v237, v239\n"
#define SW21 "v_permlane32_swap_b32 v240, v242\n"
#define SW22 "v_permlane32_swap_b32 v241, v243\n"
#define SW31 "v_permlane32_swap_b32 v244, v246\n"
#define SW32 "v_permlane32_swap_b32 v245, v247\n"
#define SW41 "v_permlane32_swap_b32 v248, v250\n"
#define SW42 "v_permlane32_swap_b32 v249, v251\n"
#define SMI "v_smfmac_f32_16x16x64_bf16 %0, %1, %2, %3"
#define SMC(M_, c_, ...) do { if ((c_) == 0) M_("v", __VA_ARGS__); else if ((c_) == 2) M_("{a[240:247]}", __VA_ARGS__); else M_("{a[248:255]}", __VA_ARGS__); SB(); } while (0)
#define SM_(bc, n, f_, w_, x_) asm volatile(SMI : "+{" AR##n "}"(ac[n]) : "v"(f_), bc(w_), "v"(x_))
#define SMP_(bc, n, f_, w_, x_, m, p) asm volatile(SW##m##p "v_smfmac_f32_16x16x64_bf16 %0, %2, %3, %4" : "+{" AR##n "}"(ac[n]), "+{" AR##m "}"(ac[m]) : "v"(f_), bc(w_), "v"(x_))
#define RC(j_) ((j_) == 1 ? 2 : ((j_) == 3 ? 3 : 0))
#define MW(n, f_, k) do { SM_(WG##n##k, n, f_, wt[n][k], ix); SB(); } while (0)
#define MO(n, k) MW(n, A[k - 2], k)
#define MR(n, f_, j_) SMC(SM_, RC(j_), n, f_, r##j_, ix)
#define MOP(n, k, m, p) do { SMP_(WG##n##k, n, A[k - 2], wt[n][k], ix, m, p); SB(); } while (0)
#define MRP(n, f_, j_, m, p) SMC(SMP_, RC(j_), n, f_, r##j_, ix, m, p)
#define SL(i_, j_) SMC(SM_, RC(j_), 0, qf[i_], r##j_, ix2)
#define SLP(i_, j_, m, p) SMC(SMP_, RC(j_), 0, qf[i_], r##j_, ix2, m, p)
#define APIN(n) asm volatile("" : "+{" AR##n "}"(ac[n]))
#define ZL() do { asm volatile("v_mov_b64 v[232:233], 0\n v_mov_b64 v[234:235], 0" : "={v[232:235]}"(ac[0])); SB(); } while (0)
#define RLO(tb_, t_, k2_) (*LP(const u32x4, (tb_) + (t_) * 4096 + (k2_) * 2048))
#define RHI(tb_, t_, k2_) (*LP(const u32x4, (tb_) + (t_) * 4096 + (k2_) * 2048 + 1024))
#define SFR(k_) (*LP(const u32x4, afb + (k_) * 1024))
#define TR1(ad_) __builtin_bit_cast(u32x2, __builtin_amdgcn_ds_read_tr16_b64_v4i16(LP(bf16x4, ad_)))
#define TR2(ad_, d_) __builtin_shufflevector(TR1(ad_), TR1((ad_) + (d_)), 0, 1, 2, 3)
#define RFR(tb_, t_, k2_) cat8(RLO(tb_, t_, k2_), RHI(tb_, t_, k2_))
#define DS(x) do { x; SB(); } while (0)
#define VV(v_, x) do { v_ = (x); PINV(v_); SB(); } while (0)
#define FLG(st_) do { asm volatile("" ::: "memory"); *LP(volatile unsigned, fl0 + (st_) * 16 + W * 4) = snv; SB(); } while (0)
#define FLGV(st_, v_) do { asm volatile("" ::: "memory"); *LP(volatile unsigned, fl0 + (st_) * 16 + W * 4) = v_; SB(); } while (0)
#define MEET() do { asm volatile("s_barrier" ::: "memory"); SB(); } while (0)
#define FRD(g_, st_) do { asm volatile("" ::: "memory"); g_ = *LP(const volatile u32x4, fl0 + (st_) * 16); asm volatile("" ::: "memory"); SB(); } while (0)
#define FCK(g_) do { mn = min3u(mn, g_.x, g_.y); mn = min3u(mn, g_.z, g_.w); PINV(mn); SB(); } while (0)
#define AD0(n_) VV(e0, ac[n_][0] + ac[n_][2])
#define AD1(n_) VV(e1, ac[n_][1] + ac[n_][3])
#define CVT(w_) VV(w_, cvtpk(e0, e1))
#define FOLD5() do { asm volatile("s_nop 7\n v_permlane32_swap_b32 v252, v254\n v_permlane32_swap_b32 v253, v255\n v_add_f32 v252, v252, v254\n v_add_f32 v253, v253, v255\n v_cvt_pk_bf16_f32 v252, v252, v253" : "+{" AR5 "}"(ac[5])); SB(); } while (0)
#define WR(ad_, w_) DS(*LP(unsigned, ad_) = w_)
#define QFR(j_) qf[j_] = TR2(qra + (j_) * 1280, 64u)
#define KMR(j_) cat8(TR2(kra + (j_) * 1024, 256u), TR2(kra + (j_) * 1024 + 512, 256u))
#define KXR(j_) RFR(kxb, 0, j_)
#pragma unroll 1
  for (int s = 0; s < 64; ++s) {
    u32x8 r0 = RFR(trq, 0, R2), r1 = RFR(trq, 1, R2), r2 = RFR(trq, 2, R2), r3 = RFR(trq, 0, 1 - R2);
    int pss = PSLOT(s); asm volatile("" : "+s"(pss));
    f32x4 ac[6] = {ZERO4, ZERO4, ZERO4, ZERO4, ZERO4, ZERO4};
    unsigned snv = (unsigned)(s + 1);
    unsigned so = xh + (unsigned)s * X1_STEP, so2 = so + 16384u + (unsigned)R2 * 1024u; so += gb; unsigned so1 = so + 1024u;
    asm volatile("" : "+v"(snv), "+s"(so), "+s"(so1), "+s"(so2));
    APIN(0); APIN(1); APIN(2); APIN(3); APIN(4); APIN(5);
    lg0 = wait_rt(tsv + (unsigned)dly);
    u32x4 g0, g1, g2; unsigned m, spins = 0;
#define G_OWN(H_) do { g0 = ld16(rs_x1, afb, so); SB(); g1 = ld16(rs_x1, afb, so1); SB(); g2 = ld16(rs_x1, afb, so2); SB(); H_; SB(); \
      MW(0, g0, 0); DS(*LP(u32x4, afb + P0 * 1024) = g0); \
      MW(1, g0, 0); VV(m, max(g0.x, g0.z)); \
      MW(2, g0, 0); DS(*LP(u32x4, afb + P1 * 1024) = g1); \
      MW(0, g1, 1); VV(m, max(max(m, g1.x), g1.z)); \
      MW(1, g1, 1); if ((W & 1) == 0) DS(*LP(u32x4, afb + (8 + R2) * 1024) = g2); VV(m, max(max(m, g2.x), g2.z)); \
      MW(2, g1, 1); } while (0)
    G_OWN(if (W == 3) { __builtin_amdgcn_raw_buffer_store_b32(min(lg0 + dur, 0xfffffffeu), rs_ts, tso, pss, 16); VMW(4);        \
      __builtin_amdgcn_raw_buffer_store_b32((unsigned)s, rs_ps, tso, 0, 16); });
    if (__builtin_expect(__ballot(m == ~0u) != 0ull, 0)) {

      const int to = lane_id() == 0 ? 128 * h : (int)0x80000000u; unsigned tn;
      for (;;) {
        __builtin_amdgcn_raw_buffer_store_b32(~0u, rs_ts, to, pss, 16);
        if (spins > 2u) __builtin_amdgcn_s_sleep(BKO);
        ac[0] = ZERO4; ac[1] = ZERO4; ac[2] = ZERO4; APIN(0); APIN(1); APIN(2);
        tn = (unsigned)__builtin_amdgcn_s_memrealtime() + *LP(const volatile unsigned, fl0 + 96);
        G_OWN((void)0);
        const bool go = spin_check(spins, tmo); if (__ballot(m == ~0u) == 0ull || !go) break; }
      __builtin_amdgcn_raw_buffer_store_b32(min(tn, 0xfffffffeu), rs_ts, to, pss, 16);
    }
    MR(0, g2, 0);
    BAR();
    u32x4 A[7], lo, hi; float e0, e1; unsigned w0, w1, w2; u32x8 r4;
    DS(A[0] = SFR(OT(0)));
    MR(1, g2, 1); DS(A[1] = SFR(OT(1)));
    MR(2, g2, 2); DS(A[2] = SFR(OT(2)));
    MW(3, g0, 0); DS(A[3] = SFR(OT(3)));
    MW(4, g0, 0); DS(A[4] = SFR(OT(4)));
    MW(5, g0, 0); DS(A[5] = SFR(OT(5)));
    MW(3, g1, 1); DS(A[6] = SFR(9 - R2));
    MW(4, g1, 1); DS(lo = RLO(trq, 1, 1 - R2));
    MW(5, g1, 1); DS(hi = RHI(trq, 1, 1 - R2)); r0 = cat8(lo, hi);
    MO(0, 2); DS(lo = RLO(trq, 2, 1 - R2));
    MO(0, 3); DS(hi = RHI(trq, 2, 1 - R2)); r1 = cat8(lo, hi);
    MO(0, 4); DS(lo = RLO(trk, 0, R2));
    MO(0, 5); DS(hi = RHI(trk, 0, R2)); r2 = cat8(lo, hi);
    MO(0, 6); DS(lo = RLO(trk, 0, 1 - R2));
    MO(0, 7); DS(hi = RHI(trk, 0, 1 - R2)); r4 = cat8(lo, hi);
    MR(0, A[6], 3);
    MR(1, A[6], 0); DS(lo = RLO(trk, 1, R2));
    MO(1, 2);
    MOP(1, 3, 0, 1);
    MOP(1, 4, 0, 2); AD0(0);
    MO(1, 5); AD1(0);
    MO(1, 6); CVT(w0);
    MO(1, 7); DS(hi = RHI(trk, 1, R2)); r3 = cat8(lo, hi);
    MR(2, A[6], 1); DS(lo = RLO(trk, 1, 1 - R2));
    MO(2, 2);
    MOP(2, 3, 1, 1);
    MOP(2, 4, 1, 2); AD0(1);
    MO(2, 5); AD1(1);
    MO(2, 6); CVT(w1);
    MO(2, 7); DS(hi = RHI(trk, 1, 1 - R2)); r0 = cat8(lo, hi);
    MR(3, g2, 2); if (W >= 2) DS(lo = RLO(trk, 2, R2));
    MR(3, A[6], 4);
    MRP(4, g2, 3, 2, 1);
    MOP(3, 2, 2, 2); AD0(2); AD1(2);
    MO(3, 3); WR(qwa, w0); WR(qwa + 1280, w1); CVT(w2); WR(qwa + 2560, w2); FLG(0);
    u32x4 qf[3], g; unsigned mn = snv;
    if (W < 2) {
      u32x4 l2;
      MO(3, 4); DS(l2 = RLO(kxb, 0, 0));
      MO(3, 5); DS(hi = RHI(kxb, 0, 0)); r4 = cat8(l2, hi);
      MO(3, 6); DS(l2 = RLO(kxb, 0, 1));
      MO(3, 7); DS(hi = RHI(kxb, 0, 1)); r3 = cat8(l2, hi);
      MR(4, A[6], 0); FRD(g, 0); DS(QFR(0));
      MO(4, 2); DS(QFR(1));
      MOP(4, 3, 3, 1); DS(QFR(2));
      MOP(4, 4, 3, 2); DS(l2 = RLO(kxb, 0, 2)); AD0(3);
      MO(4, 5); DS(hi = RHI(kxb, 0, 2)); r0 = cat8(l2, hi); AD1(3);
      MO(4, 6); VV(mn, min3u(mn, g.x, g.y)); ZL();
      MO(4, 7); VV(mn, min3u(mn, g.z, g.w));
      SL(0, 4); CVT(w0);
      SL(1, 3); DS(lo = RLO(trk, 2, R2));
      SLP(2, 0, 4, 1); DS(hi = RHI(trk, 2, R2)); r1 = cat8(lo, hi);
      MOP(5, 2, 4, 2); DS(lo = RLO(trk, 2, 1 - R2)); AD0(4);
      MO(5, 3); DS(hi = RHI(trk, 2, 1 - R2)); r2 = cat8(lo, hi); AD1(4);
      MO(5, 4); CVT(w1); AD0(0); AD1(0);
      MO(5, 5); DS(*LP(float, lwa) = e0; *LP(float, lwa + 128) = e1); FLGV(4, mn); WR(kwa, w0); WR(kwa + 1024, w1); FLG(2);
      MO(5, 6); MO(5, 7); MEET();
      MR(5, g2, 1); MR(5, A[6], 2);
      FOLD5(); WR(kwa + 2048, __float_as_uint(ac[5][0])); FLG(3); MEET();
      if (__builtin_expect(__ballot(mn != snv) != 0ull, 0)) {
        int n_ = 0; do { mn = snv; FRD(g, 0); FCK(g); } while (__ballot(mn != snv) != 0ull && ++n_ < 65536);
        QFR(0); QFR(1); QFR(2); r4 = KXR(0); r3 = KXR(1); r0 = KXR(2); ZL(); asm volatile("s_nop 1"); SL(0, 4); SL(1, 3); SL(2, 0);
        asm volatile("s_nop 7" : "+{" AR0 "}"(ac[0]));
        AD0(0); AD1(0);
        *LP(float, lwa) = e0; *LP(float, lwa + 128) = e1; FLG(4);
      }
    } else {
      MO(3, 4); DS(hi = RHI(trk, 2, R2)); r1 = cat8(lo, hi);
      MO(3, 5); DS(lo = RLO(trk, 2, 1 - R2));
      MO(3, 6); DS(hi = RHI(trk, 2, 1 - R2)); r2 = cat8(lo, hi);
      MO(3, 7);
      MR(4, A[6], 0); if (W == 2) FRD(g, 0);
      MO(4, 2);
      MOP(4, 3, 3, 1);
      MOP(4, 4, 3, 2); if (W == 2) DS(QFR(0)); AD0(3);
      MO(4, 5); if (W == 2) DS(QFR(1)); AD1(3);
      MO(4, 6); if (W == 2) DS(QFR(2)); CVT(w0);
      MO(4, 7); if (W == 2) VV(mn, min3u(mn, g.x, g.y));
      MR(5, g2, 1); if (W == 2) VV(mn, min3u(mn, g.z, g.w));
      MR(5, A[6], 2);
      MOP(5, 2, 4, 1);
      MOP(5, 3, 4, 2); AD0(4);
      MO(5, 4); AD1(4);
      MO(5, 5); WR(kwa, w0); CVT(w1); WR(kwa + 1024, w1); FLG(2);
      MO(5, 6); if (W == 2) ZL();
      MO(5, 7);
      FOLD5(); WR(kwa + 2048, __float_as_uint(ac[5][0])); FLG(3);
      MEET();
      if (W == 3) MEET();
      if (W == 2) {
      u32x4 g3; u32x2 g4; f32x4 xv;
#define XRD() do { asm volatile("" ::: "memory"); g4 = *LP(const volatile u32x2, fl0 + 64); asm volatile("" ::: "memory"); xv = *LP(const volatile f32x4, afb + L_ST); SB(); } while (0)
      XRD();
      FRD(g, 2); DS(r0 = KMR(0)); DS(r1 = KMR(1));
      MEET();
      FRD(g3, 3); DS(r2 = KMR(2));
      float ml, x0, x1, x2, x3;
      constexpr float LS = 1.4426950408889634f * 0.07216878364870323f;
#define TOKM() VV(ml, fmx(fmaxf(fmaxf(xv[0], xv[1]), xv[2]), xv[3]))
      TOKM();
      VV(x0, xv[0] - ml);
      SL(0, 0); VV(x1, xv[1] - ml);
      SL(1, 1); VV(x2, xv[2] - ml); VV(x3, xv[3] - ml); VV(x0, x0 * LS); VV(x1, x1 * LS);
      VV(mn, min3u(mn, g4.x, g4.y)); FCK(g);
      SL(2, 2);
      FCK(g3);
      VV(x0, __builtin_amdgcn_exp2f(x0)); VV(x1, __builtin_amdgcn_exp2f(x1));
      if (__builtin_expect(__ballot(mn != snv) != 0ull, 0)) {
        int n_ = 0;
        do { FRD(g3, 3); XRD(); mn = min3u(snv, g4.x, g4.y); FCK(g3); } while (__ballot(mn != snv) != 0ull && ++n_ < 65536);
        QFR(0); QFR(1); QFR(2);
        ZL(); r0 = KMR(0); r1 = KMR(1); r2 = KMR(2); SB(); SL(0, 0); SL(1, 1); SL(2, 2);
        TOKM();
        x0 = __builtin_amdgcn_exp2f(nopk(xv[0] - ml) * LS); x1 = __builtin_amdgcn_exp2f(nopk(xv[1] - ml) * LS); x2 = xv[2] - ml; x3 = xv[3] - ml;
        asm volatile("s_nop 7" : "+v"(x0), "+v"(x1), "+v"(x2), "+v"(x3));
      }
      u32x2 pt; unsigned pmw;
#define DP(c_) " " c_ " row_mask:0xf bank_mask:0xf\n"
      asm volatile("v_mul_f32 %5, %8, %5\n v_mul_f32 %6, %8, %6\n"
                   "v_add_f32_dpp v240, v232, v234 quad_perm:[0,1,2,3] row_mask:0xf bank_mask:0x3\n v_add_f32_dpp v240, v233, v235 quad_perm:[0,1,2,3] row_mask:0xf bank_mask:0xc\n"
                   "v_max_f32 v241, v240, %7\n v_exp_f32 %5, %5\n v_exp_f32 %6, %6\n"
                   "v_max_f32_dpp v241, v241, v241" DP("quad_perm:[1,0,3,2]") "v_add_f32 v242, %3, %4\n v_add_f32 v243, %5, %6\n"
                   "v_max_f32_dpp v241, v241, v241" DP("quad_perm:[2,3,0,1]") "v_add_f32 v242, v242, v243\n s_nop 0\n"
                   "v_max_f32_dpp v241, v241, v241" DP("row_half_mirror")
                   "v_sub_f32 v240, v240, v241\n v_sub_f32 v241, %7, v241\n v_mul_f32 v240, %8, v240\n v_mul_f32 v241, %8, v241\n v_exp_f32 v240, v240\n v_exp_f32 v241, v241\n s_nop 0\n"
                   "v_fma_f32 v242, v242, v241, v240\n v_mul_f32 %3, %3, v241\n v_mul_f32 %4, %4, v241\n"
                   "v_add_f32_dpp v242, v242, v242" DP("quad_perm:[1,0,3,2]") "v_mul_f32 %5, %5, v241\n v_mul_f32 %6, %6, v241\n"
                   "v_add_f32_dpp v242, v242, v242" DP("quad_perm:[2,3,0,1]") "v_mov_b32_dpp v243, v240" DP("quad_perm:[1,0,3,2]") "s_nop 0\n"
                   "v_add_f32_dpp v242, v242, v242" DP("row_half_mirror")
                   "v_rcp_f32 v242, v242\n s_nop 0\n"
                   "v_mul_f32 %3, %3, v242\n v_mul_f32 %4, %4, v242\n v_mul_f32 %5, %5, v242\n v_mul_f32 %6, %6, v242\n"
                   "v_cvt_pk_bf16_f32 v236, |%3|, |%4|\n v_cvt_pk_bf16_f32 v237, |%5|, |%6|\n"
                   "buffer_store_dwordx2 v[236:237], %9, %11, %12 offen sc1\n"
                   "v_mul_f32 v240, v240, v242\n v_mul_f32 v243, v243, v242\n v_cvt_pk_bf16_f32 %1, |v240|, |v243|\n"
                   "buffer_store_dword %1, %10, %11, %12 offen sc1"
                   : "=&{v[236:237]}"(pt), "=&v"(pmw), "+{" AR0 "}"(ac[0]), "+v"(x0), "+v"(x1), "+v"(x2), "+v"(x3)
                   : "v"(ml), "s"(lsc), "v"(po), "v"(pm), "s"(rs_p), "s"(pss) : "v240", "v241", "v242", "v243", "memory");
      SB();
      u64 t2; RT_MARK(t2);
      *LP(volatile u32x2, fl0 + 80) = (u32x2){(unsigned)t2, snv}; SB();
      }
    }
#define LWAIT(v_, off_) do { int n_ = 0; for (;;) { v_ = *LP(const volatile u32x2, fl0 + (off_)); if ((unsigned)__builtin_amdgcn_readfirstlane(v_.y) == (unsigned)(s + 1) || ++n_ >= (1 << 20)) break; __builtin_amdgcn_s_sleep(1); } asm volatile("" ::: "memory"); } while (0)
    if (W < 2) {
      u32x4 v[6], tpl; unsigned tsl;
      const int sn = s < 63 ? s + 1 : 63;
      if (W == 0) { tsl = __builtin_amdgcn_raw_buffer_load_b32(rs_ts, (lane_id() & 31) * 128, pss, 16); SB(); }
      if (__builtin_expect(tprog < (unsigned)(sn + 1), 0)) tprog = wait_t<4>(cnt + CNT(C_PT_ + h), false, (unsigned)(sn + 1), tmo);
#pragma unroll
      for (int i = 0; i < 6; ++i) v[i] = ld16(rs_kn, afb, (unsigned)sn * QK_S + (unsigned)(i * 1024));
      tpl = ld16(rs_tp, 0u, 0u);
      SB();
      if (W == 0) {
        tsv = ts_max(tsl);
        if (__builtin_expect(tsv == ~0u, 0) && s < 63) tsv = ts_wait(rs_ts, pss);
        *LP(volatile u32x2, fl0 + 104) = (u32x2){tsv, snv}; SB();
      }
#pragma unroll
      for (int i = 0; i < 6; ++i) *LP(u32x4, kxb + i * 1024) = v[i];
      tprog = (unsigned)__builtin_amdgcn_readfirstlane(min3u(tpl.x, tpl.y, tpl.z));
    }
    if (W == 3) {
      const int ln = lane_id();
      unsigned ko = (unsigned)(((ln >> 3) & 1) * 512 + (ln >> 4) * 128 + (ln & 7) * 16);
      unsigned eo = ln < 32 ? side_map(P, (unsigned)(h * 512 + ln * 16), 0u, pol) : (ln < 48 ? side_map(P, (unsigned)(16384 + (ln & 8) * 512 + h * 128 + (ln & 7) * 16), 0u, pol) : 0x80000000u);
      PINV(ko); PINV(eo); SB();
      u32x4 kv[3];
      { int n_ = 0; do { mn = snv; FRD(g, 3); FCK(g); } while (__builtin_expect(__ballot(mn != snv) != 0ull, 0) && ++n_ < 65536); }
#pragma unroll
      for (int kk = 0; kk < 3; ++kk) {
        const u32x4 v1 = TR2(kra + kk * 1024, 256u), v2 = TR2(kra + kk * 1024 + 512, 256u);
        kv[kk] = (u32x4){min(hi8 ? v2.x : v1.x, 0xfffffffeu), hi8 ? v2.y : v1.y, min(hi8 ? v2.z : v1.z, 0xfffffffeu), hi8 ? v2.w : v1.w};
      }
      asm volatile("" : "+v"(kv[0]), "+v"(kv[1]), "+v"(kv[2])); SB();
      u32x2 tt = {0u, 0u};
      if (s < 63) LWAIT(tt, 80);
      if (XSL) __builtin_amdgcn_s_sleep(XSL);
      const __amdgpu_buffer_rsrc_t rs_km = mkrs(ws + O_KM);
#pragma unroll
      for (int kk = 0; kk < 3; ++kk) st16(rs_km, ko + (unsigned)(kk * 1024), (unsigned)s * KM_S + (unsigned)h * KM_H, kv[kk]);
      if (s + PRE < 63) st16(rs_p, eo, (unsigned)PSLOT(s + PRE), (u32x4){~0u, ~0u, ~0u, ~0u});
      SB();
      tb2 = (unsigned)__builtin_amdgcn_readfirstlane(tt.x);
      dur = min(dur + 1u, tb2 - lg0); *LP(volatile unsigned, fl0 + 96) = dur + (unsigned)a.d[3];
    }
    ADAPT(dly, spins, a.d[0]);
    if (W != 0) { u32x2 tt; LWAIT(tt, 104); tsv = (unsigned)__builtin_amdgcn_readfirstlane(tt.x); }
  }
#undef trk
}

constexpr int NSELF = RSELF;
__device__ __forceinline__ void f_part(const Args& a, unsigned char* lds, const int h, const int part, const int wave, const bool isr, const unsigned tag) {
  const int lane = lane_id(), l15 = lane & 15, lq = lane >> 4;
  const unsigned lb = lds_addr(lds);
  const int kk = 4 * part + wave;
  u32x4 af[2][6];
  {
    const unsigned vo = (unsigned)((8 * (l15 >> 2) + (l15 & 3)) * 12288 + lq * 16);
    const __amdgpu_buffer_rsrc_t rv = mkrs(a.Wv + 192 * h + (size_t)(32 * kk) * 6144);
#pragma unroll
    for (int t = 0; t < 2; ++t)
#pragma unroll
      for (int e = 0; e < 6; ++e) af[t][e] = __builtin_amdgcn_raw_buffer_load_b128(rv, (int)(vo + 64u * e), 4 * t * 12288, 0);
  }
  SB();
  unsigned dv; { const unsigned r = (unsigned)(8 * wave + (lane >> 3)); dv = r * 1280u + (((unsigned)lane & 7u) ^ (((r >> 1) & 1u) * 2u + ((r >> 3) & 1u) * 4u)) * 16u; PINV(dv); }
  const int g0 = 2 * part;
#define F_COL(g_) ((g_) + g0 < 10 ? (g_) + g0 : (g_) + g0 - 10)
#define F_DMA(g_, b_) do { const u16* s_ = a.Wo + (size_t)(192 * h) * 640 + 64 * F_COL(g_); _Pragma("unroll") for (int q = 0; q < 6; ++q) glds(s_ + q * (32 * 640), dv, lb + (b_) + (unsigned)((wave + 4 * q) * 1024)); } while (0)
#define F_RA(g_) (16384u + (unsigned)((g_) < 4 ? 3 - (g_) : ((g_) < 10 ? 9 - (g_) : 0)) * 24576u)
#pragma unroll 1
  for (int g = 0; g < FD0; ++g) F_DMA(g, F_RA(g));
  unsigned xv[5];
#pragma unroll
  for (int i = 0; i < 5; ++i) { const unsigned c = (unsigned)((wave + 4 * i) * 64 + lane), r = c / 80u; xv[i] = (c ^ r) * 16u + (r >> 2) * 5120u; PINV(xv[i]); }
#define F_XD(sg_, hf_, ad_) do { _Pragma("unroll") for (int i = 0; i < 5; ++i) glds(a.Bx + (size_t)(32 * (sg_) + 4 * (hf_)) * 640, xv[i], lb + (ad_) + (unsigned)(wave * 1024 + i * 4096)); } while (0)
  const __amdgpu_buffer_rsrc_t rs = mkrs(a.ws + O_FF + (size_t)((h >> 1) * 20 + kk) * 1024);
  unsigned vo = 0x80000000u, vo1 = (unsigned)((l15 >> 3) * (16 * 20 * 1024) + (h & 1) * 512 + lq * 128 + (l15 & 7) * 16);
  unsigned bo[4], bq[4];
  { const unsigned brow = (unsigned)(8 * lq + (l15 >> 2)), bg = ((brow >> 1) & 1u) * 2u + ((brow >> 3) & 1u) * 4u;
#pragma unroll
    for (int j = 0; j < 4; ++j) { bo[j] = lb + brow * 128u + (((unsigned)(2 * j + ((l15 & 3) >> 1))) ^ bg) * 16u + (unsigned)(l15 & 1) * 8u; PINV(bo[j]); bq[j] = bo[j] + F_RA(0); } }
#define F_TR(j_, n_) __builtin_bit_cast(u32x2, __builtin_amdgcn_ds_read_tr16_b64_v4i16((LAS bf16x4*)(bq[j_] + ((n_) >> 1) * 4096 + ((n_) & 1) * 512)))
  u32x2 bf[2][12]; f32x4 acc[2][2] = {{ZERO4, ZERO4}, {ZERO4, ZERO4}};
  if (FD0 == 2) VMW(6); else if (FD0 == 3) VMW(12); else if (FD0 == 4) VMW(18); else VMW(30);
  BAR();
#pragma unroll
  for (int n = 0; n < 12; ++n) bf[0][n] = F_TR(0, n);
  unsigned so = 0;
#pragma unroll 1
  for (int g = 0; g < 10; ++g) {
#pragma unroll
    for (int j = 0; j < 4; ++j) {
      unsigned w[4];
      if (g == 0 && j < 6 - FD0) F_DMA(FD0 + j, F_RA(FD0 + j));
      if (j == 3) { VMW(24); asm volatile("s_barrier" ::: "memory");
        if (g < 4) F_DMA(g + 6, F_RA(g));
        else if (g < 8 || (isr && NSELF > 2)) F_XD((isr ? 0 : NSELF) + ((g - 4) >> 1), ~g & 1, 143360u - (unsigned)(g - 4) * 20480u);
#pragma unroll
        for (int q = 0; q < 4; ++q) { bq[q] = bo[q] + F_RA(g + 1); PINV(bq[q]); }
        SB(); }
#pragma unroll
      for (int n = 0; n < 12; ++n) {
        const int e = n >> 1, t = n & 1;
        const u32x4 b = {bf[j & 1][2 * e].x, bf[j & 1][2 * e].y, bf[j & 1][2 * e + 1].x, bf[j & 1][2 * e + 1].y};
        if (e == 0) asm volatile("v_mfma_f32_16x16x32_bf16 %0, %1, %2, 0" : "=&v"(acc[j & 1][t]) : "v"(af[t][e]), "v"(b));
        else asm volatile("v_mfma_f32_16x16x32_bf16 %0, %1, %2, %0" : "+v"(acc[j & 1][t]) : "v"(af[t][e]), "v"(b));
        SB();
        bf[(j + 1) & 1][n] = F_TR((j + 1) & 3, n); SB();
        if (n >= 3 && n < 7) { const f32x4 c = acc[(j + 1) & 1][(n - 3) >> 1]; w[n - 3] = ((n - 3) & 1) ? cvtpk(c[2], c[3]) : cvtpk(c[0], c[1]); PINV(w[n - 3]); SB(); }
        if (n == 8) { st16(rs, vo, so, (u32x4){w[0], w[1], w[2], w[3]}); vo = vo1; so = (unsigned)(4 * F_COL(g) + j) * (2u * 16u * 20u * 1024u); SB(); }
      }
    }
  }
  asm volatile("s_nop 7" : "+v"(acc[1][0]), "+v"(acc[1][1]));
  st16(rs, vo, so, (u32x4){cvtpk(acc[1][0][0], acc[1][0][1]), cvtpk(acc[1][0][2], acc[1][0][3]), cvtpk(acc[1][1][0], acc[1][1][1]), cvtpk(acc[1][1][2], acc[1][1][3])});
  init_wait(a.ws, tag);
  VMW(0);
  BAR();
  if (wave == 0 && lane < 8) __hip_atomic_fetch_add((gu32*)((unsigned*)(a.ws + O_CNT) + CNT(C_FF + (h >> 3) * 8 + lane)), 1u, RLX_AGENT);
}
#define F_WAIT(rep_) do { wait_ge<FSL>(cnt + CNT(C_FF + 8 * wave + (rep_)), 40u, tmo); } while (0)
#define F_LD(t_, k_) ld16(rs_f, (unsigned)((l15 >> 3) * 512 + lq * 128 + (l15 & 7) * 16), (unsigned)(((t_) * 20 + (k_)) * 1024))
#define FOLD(o_, a_, b_) do { \
    const float f0_ = __int_as_float(__builtin_amdgcn_update_dpp(__float_as_int(o_[0]), __float_as_int(o_[0]), 0x128, 0xc, 0xf, false)), f1_ = __int_as_float(__builtin_amdgcn_update_dpp(__float_as_int(o_[1]), __float_as_int(o_[1]), 0x128, 0xc, 0xf, false)); \
    const float f2_ = __int_as_float(__builtin_amdgcn_update_dpp(__float_as_int(o_[2]), __float_as_int(o_[2]), 0x128, 0xc, 0xf, false)), f3_ = __int_as_float(__builtin_amdgcn_update_dpp(__float_as_int(o_[3]), __float_as_int(o_[3]), 0x128, 0xc, 0xf, false)); \
    a_ = t32(f0_, f2_); b_ = t32(f1_, f3_); } while (0)

constexpr int L_PH = 10240, L_RG = 16384, RG_SLOT = 20480, LAG = RLAG, NRS = LAG + 2;
template <int W>
__device__ __forceinline__ void r_loop(const Args& a, unsigned char* lds, const int u, const unsigned pol, const u32x8 (&F)[4][10]) {
  char* ws = a.ws; asm volatile("" : "+s"(ws));
  unsigned* const cnt = (unsigned*)(ws + O_CNT); unsigned* const tmo = cnt + CNT(C_TMO);
  char* const X1 = ws + O_X1; char* const P = ws + O_PS;
  const int lane = lane_id(), l15 = lane & 15, lq = lane >> 4;
  constexpr bool FIN = W == 0;
  constexpr int wave = W, NT = 4, t0 = 4 * W;
  constexpr int NP = W == 2 ? 16 : 20, NU = W == 1 ? NU1 : (W == 2 ? 8 : 8 - NU1), U0 = W == 1 ? 0 : (W == 2 ? NU1 : NU1 + 8), NL = NP + NU;
  const __amdgpu_buffer_rsrc_t rs_x1 = mkrs(X1), rs_p = mkrs(P);
  int ix = (l15 & 2) ? 0xEEEE : 0x4444; PINV(ix);
  unsigned pt0 = (unsigned)(((l15 >> 3) * 4 + lq) * 128 + (l15 & 7) * 16);
  unsigned pmo = (unsigned)(32768 + (2 * lq + (l15 >> 3)) * 128 + (l15 & 7) * 16);
  unsigned xvo = lane < 8 ? (unsigned)(lane * 16) : 0x80000000u;
  unsigned afb = (unsigned)(L_MEM + lane * 16);
  unsigned ovo = 0x80000000u;
  unsigned tub = (unsigned)(L_RG + lq * 1024 + l15 * 16), zwb = (unsigned)(L_RG + 16384 + t0 * 256 + l15 * 16 + lq * 4);
  unsigned ga = (unsigned)(L_MEM + (lq * 16 + ((lane >> 1) & 3) * 4 + (lane & 1)) * 16 + (lane & 8));
  unsigned oob = 0x80000000u; int tsa = (lane & 31) * 128;
  if (FIN) { PINV(pt0); PINV(pmo); PINV(xvo); PINV(oob); PINV(ga); PINV(tsa); } else { PINV(ovo); PINV(tub); }
  PINV(afb); PINV(zwb);
  const __amdgpu_buffer_rsrc_t rs_uo = mkrs(ws + O_UO + (size_t)u * U_U + U0 * 1024), rs_pt = mkrs(ws + O_PT - LAG * PT_SEG), rs_out = mkrs(a.out + 640 + 8 * u);
  const unsigned* const pop = cnt + CNT(C_PO + u); const unsigned* const tkp = cnt + CNT(C_TOK + u / 20);
  const __amdgpu_buffer_rsrc_t rs_po = mkrs(pop), rs_tk = mkrs(tkp - CNT(4) * (LAG - 1));
  int dlx = a.d[1] + 2 * a.d[4], dlp = a.d[2] + a.d[4];
  const unsigned pso0 = half_of(P, 0u, 0u, pol);
  const __amdgpu_buffer_rsrc_t rs_ts = mkrs(P + pso0 + TS_OFF);
#define TK_P(g_, c_, j_, so_) ld16(rs_pt, afb - L_MEM + (unsigned)((j_) * 1024), (so_) + (unsigned)PT_BLK(g_, c_))
#define PT_AT(i_) half_of(ws + O_PT - LAG * PT_SEG, (unsigned)(i_) * PT_SEG, 0u, pol)
#define TK_A(c_, j_) ((c_) < 4 ? *(const u32x4*)(lds + tua + (j_) * 4096 + (c_) * 256) : *(const u32x4*)(lds + tza + (j_) * 1024))
#define TK_C(n_) ((n_) < 4 ? 4 : ((n_) >> 2) - 1)
#define TK_MM(g_, b_, n0_, n1_, H_) do { u32x4 ta_[8]; \
      _Pragma("unroll") for (int n = (n0_); n < (n0_) + 8 && n < (n1_); ++n) ta_[n & 7] = TK_A(TK_C(n), n & 3); \
      SB(); \
      _Pragma("unroll") for (int n = (n0_); n < (n1_); ++n) { to = mfma(ta_[n & 7], tp[(b_) + n], n == 0 ? ZERO4 : to); asm volatile("" : "+a"(to)); \
        if (n + 8 < (n1_)) ta_[n & 7] = TK_A(TK_C(n + 8), n & 3); SB(); H_; } } while (0)
#define TK_PK(o_) do { float ra, rb; FOLD(to, ra, rb); u32x4 w; ROW16(w, cvtpk(ra, rb), k0_); o_ = (u32x4){w.x, w.z, w.y, w.w}; PINV(o_); } while (0)
#define TK_ST(g_, o_, v_) __builtin_amdgcn_raw_buffer_store_b128(o_, rs_out, (int)(v_), (int)(oso + (unsigned)((g_) * 20480)), 0)
#define P_A(i_) (*(const u32x4*)(lds + upa + (i_) * 1024))
#define PH_SET(k_) do { asm volatile("" ::: "memory"); *(volatile LAS unsigned*)(LAS unsigned char*)(lds + L_PH) = (unsigned)(3 * s + (k_)); asm volatile("" ::: "memory"); } while (0)
#define PH_WAIT(k_) do { unsigned n_ = 0; while ((unsigned)__builtin_amdgcn_readfirstlane(*(volatile LAS unsigned*)(LAS unsigned char*)(lds + L_PH)) < (unsigned)(3 * s + (k_)) && ++n_ < 4096u) __builtin_amdgcn_s_sleep(1); asm volatile("" ::: "memory"); } while (0)
#define P_PASS() do { \
        _Pragma("unroll") for (int i = 0; i < 16; ++i) { p[i] = ld16(rs_p, pt0 + 1024u * (i & 3), (i >> 2) == 0 ? pso : ((i >> 2) == 1 ? ps1 : ((i >> 2) == 2 ? ps2 : ps3))); SB(); } \
        _Pragma("unroll") for (int i = 0; i < 4; ++i) { p[16 + i] = ld16(rs_p, pmo + 1024u * i, pso); SB(); } \
        PH_SET(1); \
        _Pragma("unroll") for (int i = 0; i < NPA; ++i) up[i] = P_A(i); \
        SB(); \
        asm volatile("s_nop 1\n v_mfma_f32_16x16x32_bf16 %0, %1, %2, 0" : "=&{v[252:255]}"(o) : "v"(up[0]), "v"(p[0])); up[0] = P_A(NPA); SB(); \
        _Pragma("unroll") for (int i = 1; i < 19; ++i) { asm volatile("v_mfma_f32_16x16x32_bf16 %0, %1, %2, %0" : "+{v[252:255]}"(o) : "v"(up[i % NPA]), "v"(p[i])); if (i + NPA < 20) up[i] = P_A(i + NPA); SB(); } \
          \
        asm volatile("v_mfma_f32_16x16x32_bf16 %0, %3, %4, %0\n s_nop 7\n" \
                     "v_mov_b32_dpp v252, v252 row_ror:8 row_mask:0xc bank_mask:0xf\n v_mov_b32_dpp v254, v254 row_ror:8 row_mask:0xc bank_mask:0xf\n" \
                     "v_mov_b32_dpp v253, v253 row_ror:8 row_mask:0xc bank_mask:0xf\n v_mov_b32_dpp v255, v255 row_ror:8 row_mask:0xc bank_mask:0xf\n" \
                     "v_permlane32_swap_b32 v252, v254\n v_add_f32 v254, v252, v254\n v_permlane32_swap_b32 v253, v255\n v_add_f32 v255, v253, v255\n" \
                     "v_cmp_u_f32 %1, v254, v255\n v_cvt_pk_bf16_f32 v252, v254, v255\n v_cvt_pk_bf16_f32 v253, v254, v255\n v_cndmask_b32 %2, %5, %6, %1\n s_nop 0\n" \
                     "v_permlane16_swap_b32 v252, v253\n s_nop 1\n v_permlane32_swap_b32 v252, v254\n v_permlane32_swap_b32 v253, v255\n" \
                     "buffer_store_dwordx4 v[252:255], %2, %7, %8 offen sc1\n s_nop 1" \
                     : "+{v[252:255]}"(o), "=&s"(nan), "=&v"(xs) : "v"(up[19 % NPA]), "v"(p[19]), "v"(xvo), "v"(oob), "s"(rs_x1), "s"(xo_u) : "memory"); } while (0)
#define ROW16(w_, pk_, PK2_) do { unsigned k0_ = pk_; asm("" : "+v"(k0_)); float r2_ = ra; asm("" : "+v"(r2_)); const v2u_t c_ = __builtin_amdgcn_permlane16_swap(k0_, PK2_, false, false); \
        const v2u_t d_ = __builtin_amdgcn_permlane32_swap(c_[0], __float_as_uint(ra), false, false), e_ = __builtin_amdgcn_permlane32_swap(c_[1], __float_as_uint(rb), false, false); \
        w_ = (u32x4){d_[0], e_[0], d_[1], e_[1]}; } while (0)
  unsigned rc = 0, rn = RG_SLOT, ro = 2 * RG_SLOT;
  constexpr int NPA = 10;
  f32x4 to;
  u32x4 tp[NP], un[NU > 0 ? NU : 1];
#define R_Z(H_) do { u32x4 f[4]; f[0] = AFR(0); f[1] = AFR(1); f[2] = AFR(2); \
      _Pragma("unroll") for (int k = 0; k < 10; ++k) { \
        if (k + 3 < 10) f[(k + 3) % 4] = AFR(k + 3); \
        _Pragma("unroll") for (int t = 0; t < NT; ++t) { if (t < 3) asm volatile(SMI : "+v"(z[t]) : "v"(f[k % 4]), "a"(F[t][k]), "v"(ix)); else asm volatile(SMI : "+v"(z[t]) : "v"(f[k % 4]), "v"(F[t][k]), "v"(ix)); } \
        H_; SB(); } \
      asm volatile("s_nop 7" : "+v"(z[0]), "+v"(z[1]), "+v"(z[2]), "+v"(z[3])); \
      _Pragma("unroll") for (int t = 0; t < NT; ++t) *(unsigned*)(lds + zwa + t * 256) = cvtpk(nopk(z[t][0] + z[t][2]), nopk(z[t][1] + z[t][3])); \
      SB(); } while (0)
#define RING_ON() do { rc = rn; rn = rn == (NRS - 1) * RG_SLOT ? 0u : rn + RG_SLOT; ro = ro == (NRS - 1) * RG_SLOT ? 0u : ro + RG_SLOT; } while (0)
  if (FIN) {
    const unsigned xu = (unsigned)((u >> 6) * 16384 + (u & 63) * 128);
    unsigned xo = half_of(X1, 0u, 0u, pol), psp = 0;
    u32x4 gv[10];
#define R_GLD() do { unsigned xo2 = xo + 4096u, xo3 = xo + 16384u; _Pragma("unroll") for (int k = 0; k < 10; ++k) { gv[k] = ld16(rs_x1, afb - L_MEM + (unsigned)((k & 3) * 1024), k < 4 ? xo : (k < 8 ? xo2 : xo3)); SB(); } } while (0)
#define R_GST() do { m = 0u; _Pragma("unroll") for (int k = 0; k < 10; ++k) { *(u32x2*)(lds + gx + k * 1024) = (u32x2){gv[k].x, gv[k].y}; *(u32x2*)(lds + gx + k * 1024 + 32) = (u32x2){gv[k].z, gv[k].w}; m = max(m, max(gv[k].x, gv[k].z)); SB(); } } while (0)
#pragma unroll
    for (int k = 0; k < 10; ++k) gv[k] = (u32x4){0u, 0u, 0u, 0u};
    unsigned gx = ga + (unsigned)(L_RG + 4 * RG_SLOT);
#pragma unroll 1
    for (int s = 0;; ++s) {
      f32x4 z[NT];
#pragma unroll
      for (int t = 0; t < NT; ++t) { z[t] = ZERO4; PINV(z[t]); }
      unsigned zwa = zwb + rc, upa = afb + (unsigned)(L_RG - L_MEM) + rc; PINV(zwa); PINV(upa);
      unsigned pso = pso0 + (unsigned)PSLOT(s), xo_u = xo + X1_STEP + xu;
      unsigned ps1 = pso + 4096u, ps2 = pso + 16384u, ps3 = pso + 20480u; asm volatile("" : "+s"(pso), "+s"(ps1), "+s"(ps2), "+s"(ps3), "+s"(xo_u));
      ADAPT(dlp, psp, a.d[2]);
      RING_ON();
      st16(rs_x1, s + PRE + 1 < 64 ? xvo : oob, xo_u + PRE * X1_STEP, (u32x4){~0u, ~0u, ~0u, ~0u});
      SB();
      unsigned m, gsp = 0;
      R_GST();
      if (__builtin_expect(__ballot(m == ~0u) != 0ull, 0)) {
        for (;;) { R_GLD(); R_GST(); const bool go = spin_check(gsp, tmo); if (__ballot(m == ~0u) == 0ull || !go) break; }
      }
      const unsigned tsl = __builtin_amdgcn_raw_buffer_load_b32(rs_ts, tsa, PSLOT(s), 16); SB();
      BAR();
      R_Z((void)0);
      BAR();
      ADAPT(dlx, gsp, a.d[1]);
      unsigned tsv = ts_max(tsl);
      asm volatile("" : "+s"(tsv), "+s"(dlx));
      if (s == 63) break;
      if (__builtin_expect(tsv == ~0u, 0)) tsv = ts_wait(rs_ts, PSLOT(s));
      const unsigned lg0 = wait_rt(tsv + (unsigned)dlp);
      {
        u32x4 p[20], up[NPA], o; u64 nan; unsigned xs;
        P_PASS();
        SB();
        psp = 0;
        if (__builtin_expect((nan & 0x00ff00ff00ff00ffull) != 0ull, 0)) {

          for (;;) {
            P_PASS();
            unsigned bad = 0u;
#pragma unroll
            for (int i = 0; i < 20; ++i) bad |= (p[i].x | p[i].y) | (p[i].z | p[i].w);
            const bool go = spin_check(psp, tmo);
            if (__all((int)bad >= 0) || !go) break;
          }
          st16(rs_x1, xvo, xo_u, (u32x4){min(o.x, 0xfffffffeu), o.y, min(o.z, 0xfffffffeu), o.w});
        }
      }
      PH_SET(2);
      xo += X1_STEP; gx = ga;
      const unsigned tg = wait_rt(tsv + (unsigned)dlx);
      R_GLD();
      PH_SET(3);
    }
    ro = (63 - LAG) % NRS * RG_SLOT;
  } else {
#define R_NEXT(a_, b_, uso, pts) do { _Pragma("unroll") for (int n = (a_); n < (b_); ++n) { const int q = n - NP, n0 = n - 12; \
      if (q >= 0) un[q < 0 ? 0 : q] = ld16(rs_uo, afb - L_MEM + (unsigned)((q & 3) * 1024), uso + (unsigned)((q >> 2) * 4096)); \
      else if (W == 1 && n0 >= 0) tp[q < 0 ? n : 0] = TK_P(0, TK_C(n0), n0 & 3, pts); \
      else tp[q < 0 ? n : 0] = TK_P(W, TK_C(n), n & 3, pts); \
      SB(); } } while (0)
    u32x4 o1, o0; unsigned usp = U_S, ptp = PT_AT(0);
#pragma unroll
    for (int j = 0; j < NP; ++j) tp[j] = (u32x4){0u, 0u, 0u, 0u};
#pragma unroll
    for (int j = 0; j < NU; ++j) un[j] = (u32x4){0u, 0u, 0u, 0u};
#pragma unroll 1
    for (int s = 0;; ++s) {
      f32x4 z[NT];
#pragma unroll
      for (int t = 0; t < NT; ++t) { z[t] = ZERO4; PINV(z[t]); }
      unsigned tkl; u32x4 opl;
      unsigned zwa = zwb + rc, tua = tub + ro, tza = afb + (unsigned)(L_RG - L_MEM + 16384) + ro, uwa = afb + (unsigned)(L_RG - L_MEM + U0 * 1024) + rn; PINV(zwa); PINV(tua); PINV(tza); PINV(uwa);
      int sg = s - LAG; asm volatile("" : "+s"(sg));
      unsigned oso = (unsigned)(sg < 0 ? 0 : sg) * 81920u, uso = (unsigned)(s < 62 ? s + 2 : 63) * U_S, pts = PT_AT(s + 1); asm volatile("" : "+s"(oso), "+s"(uso), "+s"(pts));
      SB();
      BAR();
      R_Z(if (RN3 + k < NL) R_NEXT(RN3 + k, RN3 + k + 1, usp, ptp); if (k == TSK) { opl = ld16(rs_po, 0u, 0u); tkl = __builtin_amdgcn_raw_buffer_load_b32(rs_tk, 0, s * 512, 16); });
      BAR();
      const unsigned ov = ovo;
      if (W == 1) {
        u32x4 ta_[8]; f32x4 t1;
#define TK_C1(n_) TK_C((n_) < 12 ? (n_) : (n_) - 12)
#pragma unroll
        for (int n = 0; n < 8; ++n) ta_[n] = TK_A(TK_C1(n), n & 3);
        SB();
#pragma unroll
        for (int n = 0; n < 20; ++n) {
          if (n < 12) { t1 = mfma(ta_[n & 7], tp[n], n == 0 ? ZERO4 : t1); asm volatile("" : "+a"(t1)); } else { to = mfma(ta_[n & 7], tp[n], n == 12 ? ZERO4 : to); asm volatile("" : "+a"(to)); }
          if (n + 8 < 20) ta_[n & 7] = TK_A(TK_C1(n + 8), n & 3);
          SB(); }
        { float ra, rb, rc_, rd; FOLD(t1, ra, rb); FOLD(to, rc_, rd); u32x4 w; ROW16(w, cvtpk(ra, rb), k0_); o1 = (u32x4){w.x, w.z, w.y, w.w}; ra = rc_; rb = rd; ROW16(w, cvtpk(ra, rb), k0_); o0 = (u32x4){w.x, w.z, w.y, w.w};
          asm volatile("" : "+v"(o1), "+v"(o0)); }
      } else { TK_MM(W, 0, 0, 4 * W + 8, (void)0); TK_PK(o1); }
#define R_OUT() do { TK_ST(W, o1, ov); if (W == 1) TK_ST(0, o0, ov); SB(); } while (0)
      if (s + 1 >= NSELF) {
#pragma unroll
        for (int j = 0; j < NU; ++j) *(u32x4*)(lds + uwa + j * 1024) = un[j];
      }
      unsigned tok = (unsigned)__builtin_amdgcn_readfirstlane(tkl), oprog = (unsigned)__builtin_amdgcn_readfirstlane(min(min(opl.x, opl.y), min(opl.z, opl.w)));
      asm volatile("" : "+s"(tok), "+s"(oprog));
      if (s == 63) break;
      if (__builtin_expect(tok < 32u || oprog < (unsigned)(s + 3) || s < LAG, 0)) {
        if (s == LAG - 1) { const int ln = lane_id(); ovo = ln < 8 ? (unsigned)(ln * 2560) : 0x80000000u; }
        if (s >= LAG - 1 && tok < 32u) wait_ge<4>(tkp + CNT(4) * (s + 1 - LAG), 32u, tmo);
        if (s < 62 && s + 2 >= NSELF && oprog < (unsigned)(s + 3)) wait_u(pop, (unsigned)(s + 3), tmo);
      }
      const unsigned late = (unsigned)__builtin_amdgcn_readfirstlane(*(volatile LAS unsigned*)(LAS unsigned char*)(lds + L_PH)) > (unsigned)(3 * s) ? 1u : 0u;

      PH_WAIT(1 + late); R_NEXT(0, RN1, uso, pts);
      RING_ON();
      PH_WAIT(2 + late); R_NEXT(RN1, RN2, uso, pts);
      if (OPH == 2) R_OUT();
      PH_WAIT(3); R_NEXT(RN2, RN3, uso, pts); usp = uso; ptp = pts;
      if (OPH == 3) R_OUT();
    }
    { const unsigned ov = ovo, oso = (63 - LAG) * 81920u; R_OUT(); }
  }
  if (FIN) { const int ln = lane_id(); ovo = ln < 8 ? (unsigned)(ln * 2560) : 0x80000000u; }
#pragma unroll 1
  for (int sg = 64 - LAG; sg < 64; ++sg) {
    ro = ro == (NRS - 1) * RG_SLOT ? 0u : ro + RG_SLOT;
    wait_ge<1>(tkp + CNT(4) * sg, 32u, tmo);
#pragma unroll
    for (int n = 0; n < 4 * W + 8; ++n) tp[n] = TK_P(W, TK_C(n), n & 3, PT_AT(sg + LAG));
    unsigned tua = tub + ro, tza = afb + (unsigned)(L_RG - L_MEM + 16384) + ro; PINV(tua); PINV(tza);
    const unsigned oso = (unsigned)sg * 81920u;
    u32x4 o1; TK_MM(W, 0, 0, 4 * W + 8, (void)0); TK_PK(o1); TK_ST(W, o1, ovo);
  }
}

__device__ __forceinline__ void r_role(const Args& a, unsigned char* lds, const int u, const int wave) {
  char* ws = a.ws; asm volatile("" : "+s"(ws));
  unsigned* const cnt = (unsigned*)(ws + O_CNT); unsigned* const tmo = cnt + CNT(C_TMO);
  const int lane = lane_id(), l15 = lane & 15, lq = lane >> 4;
  if (wave == 0) *(u32x4*)(lds + L_PH) = (u32x4){0u, 0u, 0u, 0u};
  unsigned xq[2][4];
#pragma unroll
  for (int j = 0; j < 4; ++j) { xq[0][j] = lds_addr(lds) + (unsigned)(40960 + l15 * 1280 + ((4 * j + lq) ^ l15) * 16); xq[1][j] = xq[0][j] + 81920u; PINV(xq[0][j]); PINV(xq[1][j]); }
  if (wave < 2) {
#pragma unroll
    for (int k = 5 * wave, n = 0; n < 5; ++k, ++n) { const u32x4 v = ld16(mkrs(ws + O_X1), (unsigned)lane * 16u, (unsigned)((k & 7) * 1024 + (k >> 3) * 16384));
      unsigned char* d = lds + L_MEM + k * 1024 + (lq * 16 + ((lane >> 1) & 3) * 4 + (lane & 1)) * 16 + (lane & 8); *(u32x2*)d = (u32x2){v.x, v.y}; *(u32x2*)(d + 32) = (u32x2){v.z, v.w}; }
  }
  const unsigned pol = wait_ge<8>(cnt + CNT(C_POL), 1u, tmo) & 1u;
  F_WAIT(u / 20);
  const __amdgpu_buffer_rsrc_t rs_f = mkrs(ws + O_FF + (size_t)(u * 16 + 4 * wave) * 20480);
  u32x4 Fh[4][20]; f32x4 ua[2 * NSELF][4]; u32x4 xa[2 * NSELF];
  constexpr int FW = RFW;
#pragma unroll
  for (int k = 0; k < FW; ++k)
#pragma unroll
    for (int t = 0; t < 4; ++t) { Fh[t][k] = F_LD(t, k); SB(); }
#define R_XF(i_, k_) (*(const LAS u32x4*)(xq[(i_) < 2 ? 1 : 0][(k_) & 3] + ((i_) < 4 ? (i_) : (i_) - 4) * 20480 + ((k_) >> 2) * 256))
#pragma unroll
  for (int i = 0; i < 2 * NSELF; ++i) xa[i] = R_XF(i, 0);
#pragma unroll
  for (int k = 0; k < 20; ++k) {
#pragma unroll
    for (int t = 0; t < 4; ++t) {
#pragma unroll
      for (int i = 0; i < 2 * NSELF; ++i) {
        if (k == 0) { if (t < 3) asm volatile("v_mfma_f32_16x16x32_bf16 %0, %1, %2, 0" : "=&v"(ua[i][t]) : "v"(xa[i]), "a"(Fh[t][k])); else asm volatile("v_mfma_f32_16x16x32_bf16 %0, %1, %2, 0" : "=&v"(ua[i][t]) : "v"(xa[i]), "v"(Fh[t][k])); }
        else { if (t < 3) asm volatile("v_mfma_f32_16x16x32_bf16 %0, %1, %2, %0" : "+v"(ua[i][t]) : "v"(xa[i]), "a"(Fh[t][k])); else asm volatile("v_mfma_f32_16x16x32_bf16 %0, %1, %2, %0" : "+v"(ua[i][t]) : "v"(xa[i]), "v"(Fh[t][k])); }
        if (t == 3 && k + 1 < 20) xa[i] = R_XF(i, k + 1);
        SB(); }
      if (k + FW < 20) { Fh[t][k + FW] = F_LD(t, k + FW); SB(); }
    }
  }
  asm volatile("s_nop 7" : "+v"(ua[2 * NSELF - 3][3]), "+v"(ua[2 * NSELF - 2][3]), "+v"(ua[2 * NSELF - 1][3]));
  BAR();
#pragma unroll
  for (int g = 0; g < NSELF; ++g)
#pragma unroll
    for (int t = 0; t < 4; ++t)
      *(u32x4*)(lds + L_RG + g * RG_SLOT + (4 * wave + t) * 1024 + lane * 16) = (u32x4){cvtpk(ua[2 * g][t][0], ua[2 * g][t][1]), cvtpk(ua[2 * g][t][2], ua[2 * g][t][3]), cvtpk(ua[2 * g + 1][t][0], ua[2 * g + 1][t][1]), cvtpk(ua[2 * g + 1][t][2], ua[2 * g + 1][t][3])};
  u32x8 F[4][10];
#pragma unroll
  for (int t = 0; t < 4; ++t)
#pragma unroll
    for (int k = 0; k < 10; ++k) F[t][k] = cat8(Fh[t][2 * k], Fh[t][2 * k + 1]);
  if (wave == 0) r_loop<0>(a, lds, u, pol, F); else if (wave == 1) r_loop<1>(a, lds, u, pol, F); else if (wave == 2) r_loop<2>(a, lds, u, pol, F); else r_loop<3>(a, lds, u, pol, F);
}

template <bool QK>
__device__ __forceinline__ void g_role(const Args& a, unsigned char* lds, const int u, const int wave, const unsigned tag) {
  char* ws = a.ws; asm volatile("" : "+s"(ws));
  unsigned* const cnt = (unsigned*)(ws + O_CNT); unsigned* const tmo = cnt + CNT(C_TMO);
  const int lane = lane_id(), l15 = lane & 15, lq = lane >> 4;
  u32x8 F[4][10];
  const unsigned rot = QK ? (unsigned)u % 5u : 0u;
  unsigned dvo[10];
#pragma unroll
  for (int i = 0; i < 10; ++i) { const unsigned c = (unsigned)((wave + 4 * i) * 64 + lane), r = c / 80u, pc = c - r * 80u, g = (pc >> 4) + rot;
    dvo[i] = (QK ? r : 8u * ((r & 15u) >> 2) + 4u * (r >> 4) + (r & 3u)) * 1280u + ((g < 5u ? g : g - 5u) * 16u + ((pc ^ r) & 15u)) * 16u; PINV(dvo[i]); }
#define G_XD1(sg_, b_, i_) glds(a.Bx + (size_t)(32 * ((sg_) < 64 ? (sg_) : 63)) * 640, dvo[i_], lds_addr(lds) + (b_) + (unsigned)(wave * 1024 + (i_) * 4096))
#define G_XD(sg_, b_) do { _Pragma("unroll") for (int xi_ = 0; xi_ < 10; ++xi_) G_XD1(sg_, b_, xi_); } while (0)
  if (QK) {
    const char* W_ = (const char*)((u < 24 ? a.Wk : a.Wq) + 256 * (u < 24 ? u : u - 24));
    unsigned svo[7];
#pragma unroll
    for (int q = 0; q < 7; ++q) { const unsigned c = (unsigned)((wave + 4 * q) * 64 + lane), r = c >> 5, pc = c & 31u; svo[q] = r * 12288u + 16u * (pc ^ (((r & 3u) | ((r >> 1) & 4u)) << 1)); PINV(svo[q]); }
    unsigned sv7; { const unsigned c = (unsigned)((wave + 28) * 64 + lane), r = c >> 5, pc = c & 31u; sv7 = r * 12288u + 16u * (pc ^ (((r & 3u) | ((r >> 1) & 4u)) << 1)); PINV(sv7); }
#define G_WD(i_) do { const unsigned g_ = (unsigned)((i_) >> 1) + rot; const char* s_ = W_ + (size_t)((g_ < 5u ? g_ : g_ - 5u) * 128u + ((i_) & 1) * 64u) * 12288; \
      _Pragma("unroll") for (int q = 0; q < 8; ++q) glds(s_, q < 7 ? svo[q < 7 ? q : 0] : sv7, lds_addr(lds) + (unsigned)(((i_) % 3) * 32768 + (wave + 4 * q) * 1024)); SB(); } while (0)
    G_WD(0); G_WD(1); G_XD(0, 122880u); G_WD(2);
    const unsigned row = (unsigned)(8 * lq + (l15 >> 2)), sw = ((row & 3u) | ((row >> 1) & 4u)) << 1;
    unsigned tro[4];
#pragma unroll
    for (int t = 0; t < 4; ++t) { tro[t] = row * 512u + (((unsigned)(2 * (4 * wave + t) + ((l15 & 3) >> 1))) ^ sw) * 16u + (unsigned)(l15 & 1) * 8u; PINV(tro[t]); }
#pragma unroll
    for (int i = 0; i < 10; ++i) {
      if (i == 0) VMW(26); else if (i == 1) VMW(26); else if (i < 8) VMW(16); else if (i == 8) VMW(8); else { init_wait(ws, tag); VMW(0); }
      BAR();
#pragma unroll
      for (int t = 0; t < 4; ++t) F[t][i] = cat8(trrd2(lds, tro[t] + (unsigned)((i % 3) * 32768), 2048u), trrd2(lds, tro[t] + (unsigned)((i % 3) * 32768 + 16384), 2048u));
      BAR();
      if (i + 3 < 10) G_WD(i + 3);
    }
    G_XD(1, 81920u);
  } else {
    F_WAIT(4 + u / 20);
  }
  u32x4 Wf[4][20];
  const __amdgpu_buffer_rsrc_t rs_f = mkrs(ws + O_FF + (size_t)(u * 16 + 4 * wave) * 20480);
#pragma unroll
  for (int k = 0; k < 20; ++k)
#pragma unroll
    for (int t = 0; t < 4; ++t) {
      if (QK) { Wf[t][k] = (k & 1) ? hi4(F[t][k >> 1]) : lo4(F[t][k >> 1]); if (t < 3) asm volatile("" : "+a"(Wf[t][k])); else PINV(Wf[t][k]); }
      else if (k < GFW) { Wf[t][k] = F_LD(t, k); SB(); } }
  const int gw = 4 * (u < 24 ? u : u - 24) + wave, hh = gw / 3, k3 = gw - 3 * hh;
  const __amdgpu_buffer_rsrc_t rs_fl = mkrs(QK ? cnt + CNT(C_PT_ + hh) + (u < 24 ? 0 : 4) + k3 : cnt + CNT(C_PO + u) + wave);
  const __amdgpu_buffer_rsrc_t rs_o = mkrs(QK ? ws + (u < 24 ? O_KF : O_QF) + (size_t)hh * QK_H + k3 * 2048 : ws + O_UO + (size_t)u * U_U + wave * 4096);
  int flo = lane == 0 ? 0 : (int)0x80000000u; PINV(flo);
  constexpr int I0 = QK ? 0 : NSELF;
  unsigned bc = 122880u, bn = 40960u;
  const unsigned lb = lds_addr(lds);
  unsigned xr[4], xq[4], vo = 0x80000000u, fa = lb; PINV(fa);
#pragma unroll
  for (int q = 0; q < 4; ++q) { xr[q] = lb + (unsigned)(l15 * 1280 + ((4 * q + lq) ^ l15) * 16); PINV(xr[q]); xq[q] = xr[q] + bc; }
#define G_XF(rt_, k_) (*(const LAS u32x4*)(xq[(k_) & 3] + (rt_) * 20480 + ((k_) >> 2) * 256))
#define G_MI(bc_, c_, d_, x_, w_) do { if (QK) asm volatile("v_mfma_f32_16x16x32_bf16 %0, %2, %1, " c_ : "+v"(d_) : "v"(x_), bc_(w_)); else asm volatile("v_mfma_f32_16x16x32_bf16 %0, %1, %2, " c_ : "+v"(d_) : "v"(x_), bc_(w_)); } while (0)
#define G_M(k_, t_, rt_) do { if ((t_) < 3) { if ((k_) == 0) G_MI("a", "0", acc[rt_][t_], xa[(k_) & 3][rt_], Wf[t_][k_]); else G_MI("a", "%0", acc[rt_][t_], xa[(k_) & 3][rt_], Wf[t_][k_]); } \
      else { if ((k_) == 0) G_MI("v", "0", acc[rt_][t_], xa[(k_) & 3][rt_], Wf[t_][k_]); else G_MI("v", "%0", acc[rt_][t_], xa[(k_) & 3][rt_], Wf[t_][k_]); } SB(); } while (0)
#define G_PK(c_) do { const f32x4 v_ = acc[((c_) >> 1) & 1][(c_) >> 2]; const int sj = QK ? ((c_) & 2) + ((c_) >> 3) : (c_) >> 2, se = QK ? (((c_) >> 1) & 2) + ((c_) & 1) : (c_) & 3; \
      pk[sj][se] = ((c_) & 1) ? cvtpk(v_[2], v_[3]) : cvtpk(v_[0], v_[1]); PINV(pk[sj]); SB(); } while (0)
#define G_ST(j_, i_) do { \
      st16(rs_o, vo, (unsigned)(i_) * (QK ? QK_S : U_S) + (unsigned)(QK ? ((j_) >> 1) * 6144 + ((j_) & 1) * 1024 : (j_) * 1024), pk[j_]); SB(); } while (0)
  f32x4 acc[2][4]; u32x4 xa[4][2], pk[4]; unsigned m0k, fw = lb + (unsigned)wave * 4u; PINV(fw);
#pragma unroll
  for (int j = 0; j < 8; ++j) { acc[j >> 2][j & 3] = ZERO4; PINV(acc[j >> 2][j & 3]); }
#pragma unroll
  for (int c = 0; c < 4; ++c) pk[c] = (u32x4){0u, 0u, 0u, 0u};
  *(volatile LAS unsigned*)fw = 0u;
  if (QK) VMW(10);
  BAR();
  asm volatile("s_mov_b32 %0, m0" : "=s"(m0k));
#pragma unroll
  for (int k = 0; k < 2; ++k) { xa[k][0] = G_XF(0, k); xa[k][1] = G_XF(1, k); }
#define G_K(FIRST_, k_) { constexpr int k = k_; \
      _Pragma("unroll") for (int j = 0; j < 8; ++j) { \
        const int t = j >> 1, rt = j & 1, gp = k == 19 ? j : (k == 0 ? 8 + j : -1), dq = 2 * (k - 12) + (j == 6); const bool dm = k >= 12 && k < 17 && (j == 3 || j == 6); \
        if (dm) asm volatile("s_mov_b32 m0, %0" :: "s"(md + (unsigned)(dq * 4096))); \
        G_M(k, t, rt); \
        if (FIRST_ && rt == 1 && k + GFW < 20) { Wf[t][k + GFW < 20 ? k + GFW : 0] = F_LD(t, k + GFW); SB(); } \
        if (j < 2) { xa[(k + 2) & 3][j] = G_XF(j, (k + 2) % 20); SB(); } \
        _Pragma("unroll") for (int c = 0; c < 16; ++c) if (gp == 3 + c * 3 / 4) G_PK(c); \
        if (k % 3 == 1 && k < 12 && j == 5) G_ST(k / 3, ip);        \
        if (k == 2 && j == 3) { xo_ = (unsigned)min(i + 2, 63) * 40960u; asm volatile("" : "+s"(xo_)); SB(); } \
        if (k == 2 && j == 4) { xs = (const u16*)((const char*)a.Bx + xo_); asm volatile("" : "+s"(xs)); SB(); } \
        if (k == 2 && j == 5) { md = lb + bn + (unsigned)wave * 1024u; asm volatile("" : "+s"(md)); bn = bc; SB(); } \
        if (k == 2 && j == 6) { bc = bc == 40960u ? 122880u : bc - 40960u; asm volatile("" : "+s"(bc)); SB(); } \
        if (k == 3 && j == 3) VV(iv, (unsigned)(i + 1)); \
        if (k == 3 && j == 5) VV(iw, (unsigned)i); \
        if (k == 6 && j == 3) { if (FIRST_) ; else VMW(2); *(volatile LAS unsigned*)fw = iv; SB(); } \
        if (k == 10 && j == 3) { fg = *(const volatile LAS u32x4*)fa; SB(); } \
        if (k == 11 && j == 2) VV(mn, min(fg.x, fg.y)); \
        if (k == 11 && j == 3) VV(mn, min3u(mn, fg.z, fg.w)); \
        if (k == 11 && j == 4) { \
          if (__builtin_expect(__ballot(mn < iv) != 0ull, 0)) { int n_ = 0; do { fg = *(const volatile LAS u32x4*)fa; mn = min(min3u(fg.x, fg.y, fg.z), fg.w); } while (__ballot(mn < iv) != 0ull && ++n_ < (1 << 22)); } \
          asm volatile("" ::: "memory"); SB(); } \
        if (dm) { asm volatile("global_load_lds_dwordx4 %0, %1" :: "v"(dvo[dm ? dq : 0]), "s"(xs) : "memory"); SB(); } \
        if (k == 17 && j == 2) { VMW(10); __builtin_amdgcn_raw_buffer_store_b32(iw, rs_fl, flo, 0, 16); SB(); }        \
        if (k == 17 && j >= 4) { xq[j - 4] = xr[j - 4] + bc; PINV(xq[j - 4]); SB(); } \
      } }
#define G_K4(F_, k_) G_K(F_, k_) G_K(F_, k_ + 1) G_K(F_, k_ + 2) G_K(F_, k_ + 3)
#define G_SEG(F_) do { const u16* xs; unsigned xo_, md, iv, iw, mn; u32x4 fg; G_K4(F_, 0) G_K4(F_, 4) G_K4(F_, 8) G_K4(F_, 12) G_K4(F_, 16) } while (0)
#define G_FLUSH(i_) do { asm volatile("s_nop 7" : "+v"(acc[1][1]), "+v"(acc[0][2]), "+v"(acc[1][2]), "+v"(acc[0][3]), "+v"(acc[1][3])); \
      _Pragma("unroll") for (int c = 7; c < 16; ++c) G_PK(c); \
      _Pragma("unroll") for (int j = 0; j < 4; ++j) G_ST(j, i_); } while (0)
#define G_FIRST() do { vo = (unsigned)lane_id() * 16u; G_FLUSH(I0); VMW(0); __builtin_amdgcn_raw_buffer_store_b32(I0 + 1u, rs_fl, flo, 0, 16); } while (0)
  if (!QK) { const int i = I0, ip = I0; G_SEG(true); G_FIRST(); }
#pragma unroll 1
  for (int i = QK ? I0 : I0 + 1, ip = I0; i < 64; ip = i, ++i) {
    G_SEG(false);
    if (__builtin_expect(i < I0 + (QK ? AQ0 + APN : AU0 + APU), 0)) {
      if (i == I0) G_FIRST();
      else if (i >= I0 + (QK ? AQ0 : AU0)) {
        const int n = ((QK ? (i - AQ0) * 48 : APN * 48 + (i - I0 - AU0) * 80) + u) * 32768 + wave * 8192;
        const char* src = n < 2457600 ? (const char*)a.Awqkv + n : (n < 3276800 ? (const char*)a.Awo + (n - 2457600) : (const char*)a.Ax + (n < 5898240 ? n - 3276800 : 0));
        touch(src + lane_id() * 128, lb + 256u); }
    }
  }
  G_FLUSH(63);
  asm volatile("s_mov_b32 m0, %0" :: "s"(m0k));
  VMW(0);
  __builtin_amdgcn_raw_buffer_store_b32(64u, rs_fl, flo, 0, 16);
  BAR();
}

__device__ __forceinline__ void att_role(const Args& a, const int h, const int par) {
  char* ws = a.ws;
  unsigned* const cnt = (unsigned*)(ws + O_CNT); unsigned* const tmo = cnt + CNT(C_TMO);
  const int lane = lane_id(), l15 = lane & 15, lq = lane >> 4;
  const __amdgpu_buffer_rsrc_t rs_q = mkrs(ws + O_QF + (size_t)h * QK_H), rs_k = mkrs(ws + O_KF + (size_t)h * QK_H), rs_km = mkrs(ws + O_KM + (size_t)h * KM_H), rs_pt = mkrs(ws + O_PT + (size_t)h * 128);
  const unsigned pol = wait_ge<8>(cnt + CNT(C_POL), 1u, tmo) & 1u;
  unsigned pv[5];
  { const int gl = l15 >> 3, cl = lq >> 1; const unsigned ro = (unsigned)((l15 & 7) * 16 + (lq & 1) * 8);
    pv[0] = cl <= gl ? (unsigned)PT_BLK(gl, cl) + ro : 0x80000000u; pv[1] = (unsigned)PT_BLK(2 + gl, cl) + ro; pv[2] = cl <= gl ? (unsigned)PT_BLK(2 + gl, 2 + cl) + ro : 0x80000000u;
    pv[3] = lq < 2 ? (unsigned)PT_BLK(gl, 4) + ro : 0x80000000u; pv[4] = lq < 2 ? (unsigned)PT_BLK(2 + gl, 4) + ro : 0x80000000u; }
  const float scale = 0.07216878364870323f, LOG2E = 1.4426950408889634f;
  unsigned tprog = 0;
  u32x4 qn[12], kn[12];
#define AT_LD(s_) do { if (tprog < (unsigned)((s_) + 1)) tprog = wait_t<16>(cnt + CNT(C_PT_ + h), true, (unsigned)((s_) + 1), tmo); \
    _Pragma("unroll") for (int i = 0; i < 12; ++i) { kn[i] = ld16(rs_k, (unsigned)(i * 1024 + lane * 16), (unsigned)(s_) * QK_S); qn[i] = ld16(rs_q, (unsigned)(i * 1024 + lane * 16), (unsigned)(s_) * QK_S); } SB(); } while (0)
  AT_LD(par);
#pragma unroll 1
  for (int s = par; s < 64; s += 2) {
    f32x4 sc[2][3];
    u32x4 qf[2][6];
    float mx[2], sum[2];
#pragma unroll
    for (int q = 0; q < 2; ++q)
#pragma unroll
      for (int t = 0; t <= q; ++t) {
        sc[q][t] = ZERO4;
#pragma unroll
        for (int kk = 0; kk < 6; ++kk) sc[q][t] = mfma(kn[6 * t + kk], qn[6 * q + kk], sc[q][t]);
      }
#pragma unroll
    for (int i = 0; i < 12; ++i) { qf[i / 6][i % 6] = qn[i]; PINV(qf[i / 6][i % 6]); }
    asm volatile("" : "+a"(sc[0][0]), "+a"(sc[1][0]), "+a"(sc[1][1])); SB();
    AT_LD(s < 62 ? s + 2 : s);
#pragma unroll
    for (int q = 0; q < 2; ++q) {
      mx[q] = -3.0e38f;
#pragma unroll
      for (int t = 0; t <= q; ++t)
#pragma unroll
        for (int r = 0; r < 4; ++r) {
          const float v = rbf(rbf(sc[q][t][r]) * scale);
          sc[q][t][r] = (t < q || 4 * lq + r <= l15) ? v : -3.0e38f;
          mx[q] = fmaxf(mx[q], sc[q][t][r]);
        }
      mx[q] = max32(max16(mx[q]));
      sum[q] = 0.f;
#pragma unroll
      for (int t = 0; t <= q; ++t)
#pragma unroll
        for (int r = 0; r < 4; ++r) { const float e = (t < q || 4 * lq + r <= l15) ? __builtin_amdgcn_exp2f(nopk(sc[q][t][r] - mx[q]) * LOG2E) : 0.f; sc[q][t][r] = e; sum[q] += e; }
      PINV(mx[q]); PINV(sum[q]); PINV(sc[q][0]);
    }
    PINV(sc[1][1]);
    SB();
    if (s >= 60) wait_ge<2>(cnt + CNT(C_PS + h), (unsigned)(s == 63 ? 63 : s + 1), tmo); else wait_ge<6>(cnt + CNT(C_PS + h), (unsigned)(s + 1), tmo);
    {
      u32x4 km[6];
      unsigned spins = 0;
      for (;;) {
        unsigned m = 0u;
#pragma unroll
        for (int kk = 0; kk < 6; ++kk) { km[kk] = ld16(rs_km, (unsigned)(kk * 512 + lq * 128 + (l15 & 7) * 16), (unsigned)s * KM_S); m = max(m, max(km[kk].x, km[kk].z)); }
        if (__all(m != ~0u)) break;
        __builtin_amdgcn_s_sleep(3);
        if (!spin_check(spins, tmo)) break;
      }
#pragma unroll
      for (int q = 0; q < 2; ++q) {
        sc[q][2] = ZERO4;
#pragma unroll
        for (int kk = 0; kk < 6; ++kk) sc[q][2] = mfma(km[kk], qf[q][kk], sc[q][2]);
      }
    }
    const unsigned po = half_of(ws + O_PT, (unsigned)s * PT_SEG, 0u, pol);
#pragma unroll
    for (int q = 0; q < 2; ++q) {
      float m = mx[q];
#pragma unroll
      for (int r = 0; r < 4; ++r) { sc[q][2][r] = rbf(rbf(sc[q][2][r]) * scale); m = fmaxf(m, sc[q][2][r]); }
      m = max16(m);
      const float f = __builtin_amdgcn_exp2f(nopk(mx[q] - m) * LOG2E);
      float sm = 0.f;
#pragma unroll
      for (int r = 0; r < 4; ++r) { sc[q][2][r] = __builtin_amdgcn_exp2f(nopk(sc[q][2][r] - m) * LOG2E); sm += sc[q][2][r]; }
      const float inv = __builtin_amdgcn_rcpf(sum32(sum16(__builtin_fmaf(sm, 0.5f, sum[q] * f)))), gg = f * inv;
      st8(rs_pt, pv[q], po, cvtpk(sc[q][0][0] * gg, sc[q][0][1] * gg), cvtpk(sc[q][0][2] * gg, sc[q][0][3] * gg));
      if (q == 1) st8(rs_pt, pv[2], po, cvtpk(sc[1][1][0] * gg, sc[1][1][1] * gg), cvtpk(sc[1][1][2] * gg, sc[1][1][3] * gg));
      st8(rs_pt, pv[3 + q], po, cvtpk(sc[q][2][0] * inv, sc[q][2][1] * inv), cvtpk(sc[q][2][2] * inv, sc[q][2][3] * inv));
    }
    VMW(0);
    if (lane < 4) __hip_atomic_fetch_add((gu32*)(cnt + CNT(C_TOK + 4 * s + lane)), 1u, RLX_AGENT);
  }
}

__device__ __forceinline__ float dot2bf(unsigned x, unsigned y, float c) { return __builtin_amdgcn_fdot2_f32_bf16(__builtin_bit_cast(bf16x2_t, x), __builtin_bit_cast(bf16x2_t, y), c, false); }
__device__ __forceinline__ void a_role(const Args& a, unsigned char* lds, const int j, const int rg, const int wave) {
  char* ws = a.ws; asm volatile("" : "+s"(ws));
  unsigned* const cnt = (unsigned*)(ws + O_CNT); unsigned* const tmo = cnt + CNT(C_TMO);
  const int lane = lane_id(), l15 = lane & 15, lq = lane >> 4;
  const unsigned lb = lds_addr(lds);
  constexpr int LR = 81920, RS = 288;
  const int r0 = 256 * rg - 16;
  {
    u32x4 wt[2][20];
    {
      const unsigned r = (unsigned)(4 * wave + lq), p0 = (unsigned)l15 ^ (((r & 3u) | ((r >> 1) & 4u)) << 1), p = p0 < 15u ? p0 : 14u;
      unsigned wv = r * 3840u + (p / 5u) * 1280u + (p % 5u) * 16u; PINV(wv);
#pragma unroll
      for (int q = 0; q < 40; ++q) glds(a.Awqkv + 40 * j + q * (16 * 1920), wv, lb + (unsigned)((wave + 4 * q) * 1024));
    }
    unsigned dvo[5];
#pragma unroll
    for (int i = 0; i < 5; ++i) { const unsigned c = (unsigned)((wave + 4 * i) * 64 + lane), r = c / 80u; dvo[i] = (c ^ r) * 16u; PINV(dvo[i]); }
#define A_XS(t_) (a.Ax + (size_t)max(r0 + 16 * min(t_, 16), 0) * 640)
#define A_XD1(src_, t_, i_) glds(src_, dvo[i_], lb + (unsigned)(((t_) & 3) * 20480 + wave * 1024 + (i_) * 4096))
    const unsigned row = (unsigned)(8 * lq + (l15 >> 2)), sw = ((row & 3u) | ((row >> 1) & 4u)) << 1;
    unsigned tro[2];
#pragma unroll
    for (int t = 0; t < 2; ++t) tro[t] = row * 256u + (((unsigned)(2 * (2 * wave + t) + ((l15 & 3) >> 1))) ^ sw) * 16u + (unsigned)(l15 & 1) * 8u;
#pragma unroll
    for (int i = 0; i < 5; ++i) {
      if (i == 0) VMW(32); else if (i == 1) VMW(24); else if (i == 2) VMW(16); else if (i == 3) VMW(8); else VMW(15);
      BAR();
      if (i == 3) {
#pragma unroll
        for (int q = 0; q < 15; ++q) A_XD1(A_XS(q / 5), q / 5, q % 5);
      }
#pragma unroll
      for (int t = 0; t < 2; ++t) { unsigned o_ = tro[t] + (unsigned)(i * 32768); PINV(o_);
#pragma unroll
        for (int k = 0; k < 4; ++k) wt[t][4 * i + k] = trrd2(lds, o_ + (unsigned)(k * 8192), 1024u); }
      SB();
    }
    unsigned xr[4], xq[4], wo_[2];
#pragma unroll
    for (int q = 0; q < 4; ++q) { xr[q] = lb + (unsigned)(l15 * 1280 + ((4 * q + lq) ^ l15) * 16); PINV(xr[q]); xq[q] = xr[q]; }
#pragma unroll
    for (int t = 0; t < 2; ++t) { const int v = 8 * wave + 4 * t + lq; wo_[t] = lb + (unsigned)(LR + l15 * RS + (v < 30 ? (v / 5) * 48 + (v % 5) * 8 : (v - 30) * 48 + 40)); }
    unsigned fa = lb + 163824u, fw = fa + (unsigned)wave * 4u; PINV(fa); PINV(fw);
    BAR();
    *(volatile LAS unsigned*)fw = 0u;
    VMW(10);
    BAR();
#define A_XF(k_) (*(const LAS u32x4*)(xq[(k_) & 3] + ((k_) >> 2) * 256))
    f32x4 acc[2][2] = {{ZERO4, ZERO4}, {ZERO4, ZERO4}}; u32x4 xa[4];
#pragma unroll
    for (int k = 0; k < 3; ++k) xa[k] = A_XF(k);
#define A_TILE(T_, P_) do { const u16* xs = A_XS((T_) + 3); unsigned md = lb + (unsigned)((((T_) + 3) & 3) * 20480 + wave * 1024), iv, mn; asm volatile("" : "+s"(xs)); asm volatile("" : "+s"(md)); u32x2 pw[2]; u32x4 fg; \
      _Pragma("unroll") for (int k = 0; k < 20; ++k) { \
        _Pragma("unroll") for (int t = 0; t < 2; ++t) { const bool dm = t == 1 && k >= 11 && k < 16; \
          if (dm) asm volatile("s_mov_b32 m0, %0" :: "s"(md + (unsigned)((k - 11) * 4096))); \
          if (k == 0) asm volatile("v_mfma_f32_16x16x32_bf16 %0, %1, %2, 0" : "=&v"(acc[P_][t]) : "v"(wt[t][k]), "v"(xa[k & 3])); \
          else asm volatile("v_mfma_f32_16x16x32_bf16 %0, %1, %2, %0" : "+v"(acc[P_][t]) : "v"(wt[t][k]), "v"(xa[k & 3])); \
          SB(); \
          if (t == 0) { xa[(k + 3) & 3] = A_XF((k + 3) % 20); SB(); } \
          if (t == 1 && k == 0) VV(iv, (unsigned)((T_) + 1)); \
          if (t == 1 && k == 1) { VMW(5); *(volatile LAS unsigned*)fw = iv; SB(); }        \
          if (t == 1 && (k == 2 || k == 3)) { pw[k & 1] = (u32x2){cvtpk(acc[1 - (P_)][k & 1][0], acc[1 - (P_)][k & 1][1]), cvtpk(acc[1 - (P_)][k & 1][2], acc[1 - (P_)][k & 1][3])}; PINV(pw[k & 1]); SB(); } \
          if (t == 1 && k == 4) { *(LAS u32x2*)wo_[0] = pw[0]; *(LAS u32x2*)wo_[1] = pw[1]; SB(); } \
          if (t == 1 && k == 5) { wo_[0] += wst; wo_[1] += wst; wst = 16 * RS; PINV(wo_[0]); PINV(wo_[1]); SB(); } \
          if (t == 1 && k == 6) { fg = *(const volatile LAS u32x4*)fa; SB(); } \
          if (t == 1 && k == 9) VV(mn, min(min3u(fg.x, fg.y, fg.z), fg.w)); \
          if (t == 1 && k == 10) { \
            if (__builtin_expect(__ballot(mn < iv) != 0ull, 0)) { int n_ = 0; do { fg = *(const volatile LAS u32x4*)fa; mn = min(min3u(fg.x, fg.y, fg.z), fg.w); } while (__ballot(mn < iv) != 0ull && ++n_ < (1 << 22)); } \
            asm volatile("" ::: "memory"); SB(); } \
          if (dm) { asm volatile("global_load_lds_dwordx4 %0, %1" :: "v"(dvo[dm ? k - 11 : 0]), "s"(xs) : "memory"); SB(); } \
          if (t == 0 && k >= 13 && k < 17) { xq[(k + 3) & 3] = xr[(k + 3) & 3] + (unsigned)((((T_) + 1) & 3) * 20480); PINV(xq[(k + 3) & 3]); SB(); } \
        } } } while (0)
    unsigned m0k; asm volatile("s_mov_b32 %0, m0" : "=s"(m0k));
    unsigned wst = 0;
#pragma unroll 1
    for (int T = 0; T < 16; T += 2) { A_TILE(T, 0); A_TILE(T + 1, 1); }
    A_TILE(16, 0);
    asm volatile("s_mov_b32 m0, %0" :: "s"(m0k));
    asm volatile("s_nop 7" : "+v"(acc[0][0]), "+v"(acc[0][1]));
#pragma unroll
    for (int t = 0; t < 2; ++t) *(LAS u32x2*)wo_[t] = (u32x2){cvtpk(acc[0][t][0], acc[0][t][1]), cvtpk(acc[0][t][2], acc[0][t][3])};
  }
  VMW(0);
  BAR();
  const int jc = j & 7;
  unsigned ov_[5];
#pragma unroll
  for (int i = 0; i < 5; ++i) { const unsigned c = (unsigned)((wave + 4 * i) * 64 + lane), r = c / 10u; ov_[i] = r * 1280u + (c - r * 10u) * 16u; PINV(ov_[i]); }
#define A_OD(g_) do { _Pragma("unroll") for (int i = 0; i < 5; ++i) glds(a.Awo + 80 * jc + (g_) * (128 * 640), ov_[i], lb + (unsigned)((g_) * 20480 + (wave + 4 * i) * 1024)); } while (0)
  A_OD(0); A_OD(1); A_OD(2); A_OD(3);
  const __amdgpu_buffer_rsrc_t rs_oa = mkrs(ws + O_OA);
#pragma unroll 1
  for (int ps = 0; ps < 2; ++ps) {
    const int rl = 32 * (2 * wave + ps) + (lane & 31), row = 256 * rg + rl, hh = lane >> 5;
    unsigned ra[8];
#pragma unroll
    for (int w = 0; w < 8; ++w) ra[w] = (unsigned)(LR + 48 * hh + (rl + 16 - 2 * w) * RS);
#define A_LD10(d_, ad_) do { const u32x4 x0 = *(const u32x4*)(lds + (ad_)), x1 = *(const u32x4*)(lds + (ad_) + 16); const u32x2 x2 = *(const u32x2*)(lds + (ad_) + 32); \
        d_[0] = x0.x; d_[1] = x0.y; d_[2] = x0.z; d_[3] = x0.w; d_[4] = x1.x; d_[5] = x1.y; d_[6] = x1.z; d_[7] = x1.w; d_[8] = x2.x; d_[9] = x2.y; } while (0)
      unsigned q[10], kv[8][10], vv[8][10];
      A_LD10(q, ra[0]);
#pragma unroll
      for (int w = 0; w < 8; ++w) A_LD10(kv[w], ra[w] + 96);
#pragma unroll
      for (int w = 0; w < 8; ++w) A_LD10(vv[w], ra[w] + 192);
      SB();
      float lg[8], mx = -3.0e38f;
#pragma unroll
      for (int w = 0; w < 8; ++w) lg[w] = 0.f;
#pragma unroll
      for (int i = 0; i < 10; ++i)
#pragma unroll
        for (int w = 0; w < 8; ++w) lg[w] = dot2bf(q[i], kv[w][i], lg[w]);
#pragma unroll
      for (int w = 0; w < 8; ++w) { lg[w] = row - 2 * w >= 0 ? rbf(rbf(lg[w]) * 0.22360679774997896f) : rbf(-1000000000.0f); mx = fmaxf(mx, lg[w]); }
      float sum = 0.f;
#pragma unroll
      for (int w = 0; w < 8; ++w) { lg[w] = __expf(lg[w] - mx); sum += lg[w]; }
      const float inv = __builtin_amdgcn_rcpf(sum);
      float ov[20];
#pragma unroll
      for (int d = 0; d < 20; ++d) ov[d] = 0.f;
#pragma unroll
      for (int w = 0; w < 8; w += 2) {
        const unsigned pw = cvtpk(lg[w] * inv, lg[w + 1] * inv);
#pragma unroll
        for (int i = 0; i < 10; ++i) {
          ov[2 * i] = dot2bf(__builtin_amdgcn_perm(vv[w + 1][i], vv[w][i], 0x05040100u), pw, ov[2 * i]);
          ov[2 * i + 1] = dot2bf(__builtin_amdgcn_perm(vv[w + 1][i], vv[w][i], 0x07060302u), pw, ov[2 * i + 1]);
        }
      }
    unsigned pk[10];
#pragma unroll
    for (int i = 0; i < 10; ++i) pk[i] = cvtpk(ov[2 * i], ov[2 * i + 1]);
    const v2u_t m0 = __builtin_amdgcn_permlane32_swap(pk[0], pk[0], false, false), m1 = __builtin_amdgcn_permlane32_swap(pk[1], pk[1], false, false);
    const unsigned oo = (unsigned)((row >> 4) * 20480 + (row & 15) * 16 + (5 * j + 3 * hh) * 256);
    st16(rs_oa, oo, 0u, hh ? (u32x4){pk[2], pk[3], pk[4], pk[5]} : (u32x4){pk[0], pk[1], pk[2], pk[3]});
    st16(rs_oa, oo + 256u, 0u, hh ? (u32x4){pk[6], pk[7], pk[8], pk[9]} : (u32x4){pk[4], pk[5], pk[6], pk[7]});
    st16(rs_oa, hh ? 0x80000000u : oo + 512u, 0u, (u32x4){pk[8], pk[9], m0[1], m1[1]});
  }
  VMW(0);
  BAR();
  if (wave == 3 && lane == 0) __hip_atomic_fetch_add((gu32*)(cnt + CNT(C_AO + rg)), 1u, RLX_AGENT);
  A_OD(4);
  u32x4 wt[5][20];
  {
    unsigned o_ = (unsigned)((8 * lq + (l15 >> 2)) * 160 + 8 * (l15 & 3)), o2 = o_ + 51200u; PINV(o_); PINV(o2);
#pragma unroll
    for (int kk = 0; kk < 20; ++kk) {
      if (kk == 16) { VMW(0); BAR(); }
#pragma unroll
      for (int t = 0; t < 5; ++t) wt[t][kk] = trrd2(lds, (kk < 10 ? o_ : o2) + (unsigned)((kk % 10) * 5120 + 32 * t), 640u);
      SB();
    }
  }
  wait_ge<4>(cnt + CNT(C_AO + rg), 16u, tmo);
  const int rt0 = 16 * rg + 8 * (j >> 3) + 2 * wave;
  const __amdgpu_buffer_rsrc_t rs_of = mkrs(ws + O_OA + (size_t)rt0 * 20480), rs_out = mkrs(a.out + (size_t)rt0 * (16 * 1280) + 80 * jc);
  unsigned fo = (unsigned)lane * 16u; PINV(fo);
  u32x4 of[16];
#pragma unroll
  for (int n = 0; n < 16; ++n) { of[n] = ld16(rs_of, fo, (unsigned)(n * 1024)); SB(); }
  f32x4 acc[5];
#pragma unroll
  for (int n = 0; n < 40; ++n) {
    const int k = n % 20;
#pragma unroll
    for (int t = 0; t < 5; ++t) {
      if (k == 0) { if (t < 4) asm volatile("v_mfma_f32_16x16x32_bf16 %0, %1, %2, 0" : "=&v"(acc[t]) : "a"(wt[t][k]), "v"(of[n & 15])); else asm volatile("v_mfma_f32_16x16x32_bf16 %0, %1, %2, 0" : "=&v"(acc[t]) : "v"(wt[t][k]), "v"(of[n & 15])); }
      else { if (t < 3 || (t == 3 && k < 3)) asm volatile("v_mfma_f32_16x16x32_bf16 %0, %1, %2, %0" : "+v"(acc[t]) : "a"(wt[t][k]), "v"(of[n & 15])); else asm volatile("v_mfma_f32_16x16x32_bf16 %0, %1, %2, %0" : "+v"(acc[t]) : "v"(wt[t][k]), "v"(of[n & 15])); }
    }
    SB();
    if (n + 16 < 40) { of[n & 15] = ld16(rs_of, fo, (unsigned)((n + 16) * 1024)); SB(); }
    if (k == 19) {
      asm volatile("s_nop 7" : "+v"(acc[0]), "+v"(acc[1]), "+v"(acc[2]), "+v"(acc[3]), "+v"(acc[4]));
      const int ln = lane_id();
#pragma unroll
      for (int t = 0; t < 5; ++t) { const u32x2 w = {cvtpk(acc[t][0], acc[t][1]), cvtpk(acc[t][2], acc[t][3])}; __builtin_amdgcn_raw_buffer_store_b64(w, rs_out, (ln & 15) * 2560 + (ln >> 4) * 8 + 32 * t, (n / 20) * (16 * 2560), 0); }
      SB();
    }
  }
}

__device__ __forceinline__ void calibrate(char* ws, const u16* mem0, const int id, const unsigned tag) {
  unsigned* const cnt = (unsigned*)(ws + O_CNT);
  const int lane = lane_id();
  if (id < 20) {
    const int k = id >> 1;
    const u32x4 v = *(const u32x4*)(mem0 + (lane & 7) * 640 + 64 * k + (lane >> 3) * 8);
    st16(mkrs(ws + O_X1), (unsigned)(lane * 16), (unsigned)((k & 7) * 1024 + (k >> 3) * 16384 + (id & 1) * 8192), (u32x4){min(v.x, 0xfffffffeu), v.z, min(v.y, 0xfffffffeu), v.w});
  }
  {
    const u32x4 ones = {~0u, ~0u, ~0u, ~0u}; const unsigned vo = (unsigned)lane * 16u;
    if (id < 60) st16(mkrs(cnt), vo, (unsigned)id * 1024u, (u32x4){0u, 0u, 0u, 0u});
    { const unsigned ko = 63u * KM_S + ((unsigned)id >> 1) * KM_H + ((unsigned)id & 1u) * 1024u; st16(mkrs(ws + O_KM), vo, ko, ones); st16(mkrs(ws + O_KM), vo, ko + 1024u, ones); }
    const unsigned sl = (unsigned)id >> 3, e = (unsigned)id & 7u;
    static_assert(PRE == 8, "");
#pragma unroll
    for (int i = 0; i < 6; ++i) st16(mkrs(ws + O_PS), vo, sl * 65536u + (e * 6u + (unsigned)i) * 1024u, ones);
#pragma unroll
    for (int i = 0; i < 2; ++i) st16(mkrs(ws + O_X1), vo, (sl + 1u) * X1_STEP + (e * 2u + (unsigned)i) * 1024u, ones);
    if (e < 4u) st16(mkrs(ws + O_X1), vo, (sl + 1u) * X1_STEP + 16384u + (e >> 1) * 8192u + (e & 1u) * 1024u, ones);
    VMW(0);
    if (lane == 0) __hip_atomic_store((gu32*)(cnt + W_DONE + id), tag, RLX_AGENT);
  }
  char* cal = ws + O_CAL + (id & 3) * 16384 + (id >> 2) * 512;
  const unsigned flip = ((unsigned)((size_t)cal >> 21) ^ xcc_side()) & 1u;
  unsigned best[2] = {65535u, 65535u}, acc = 0;
#pragma unroll 1
  for (int r = (id & 4) ? CALN : 0; r < CALN; ++r) {
#pragma unroll
    for (int hf = 0; hf < 2; ++hf) {
      const unsigned long long t0 = __builtin_amdgcn_s_memtime();
      if (lane == 0) acc += __hip_atomic_fetch_add((gu32*)(cal + ((unsigned)hf ^ flip) * 8192u + (acc & 0u)), 1u, RLX_AGENT);
      asm volatile("s_waitcnt vmcnt(0)" ::: "memory");
      const unsigned d = (unsigned)(__builtin_amdgcn_s_memtime() - t0);
      best[hf] = min(best[hf], d);
    }
  }
  init_wait(ws, tag);
  if (lane < 2) __hip_atomic_fetch_max((gu32*)(cnt + CNT(C_FILL) + lane), 65536u - (lane ? best[1] : best[0]) + (acc & 0u), RLX_AGENT);
  VMW(0);
  const unsigned n = (unsigned)__builtin_amdgcn_readfirstlane(__hip_atomic_fetch_add((gu32*)(cnt + CNT(C_FILL) + 2), lane == 0 ? 1u : 0u, RLX_AGENT));
  if (n == 63u) {
    const u32x2 b = ld8(mkrs(cnt + CNT(C_FILL)), 0u, 0u);
    if (lane == 0) __hip_atomic_store((gu32*)(cnt + CNT(C_POL)), b.y > b.x ? 3u : 2u, RLX_AGENT);
  }
}

__global__ __launch_bounds__(256, 1) void k_mega(Args a) {
  extern __shared__ __attribute__((aligned(16))) unsigned char shm[];
  volatile LAS int* sl = (volatile LAS int*)shm;
  unsigned ep = 0;
  if (threadIdx.x < 64u) ep = __hip_atomic_load((const gu32*)(a.ws + O_CNT) + W_EPOCH + (threadIdx.x == 0u ? 0u : 128u + (threadIdx.x & 7u) * 32u), RLX_AGENT);
  else {
    const char* pc; asm volatile("s_getpc_b64 %0" : "=s"(pc)); const unsigned o = ((blockIdx.x >> 3) * 48u + ((threadIdx.x - 64u) >> 2)) * 128u;
    touch(pc + (o < (unsigned)CPF ? o : 0u), lds_addr(shm) + 256u); }
  if (threadIdx.x == 0) {
    const unsigned x0 = (unsigned)__builtin_amdgcn_s_getreg((3 << 11) | 20) & 7u;
    gu32* const cw = (gu32*)(a.ws + O_CNT);
    const unsigned first = x0 * 32u + ((blockIdx.x >> 3) & 31u);
    const unsigned tg = ep + 0x9e3779b1u;
    unsigned slot = first;
#pragma unroll 1
    for (unsigned i = 0; i < 256u; ++i) {
      slot = ((first + (i & ~31u)) & 224u) | ((first + i) & 31u);
      if (__hip_atomic_exchange(cw + W_SLOT + slot, tg, RLX_AGENT) != tg) break;
    }
    if (slot < 128u) {
      constexpr unsigned NS[4] = {LAY_S}, NA[4] = {LAY_A};
      const unsigned x = slot >> 5, k = slot & 31u;
      unsigned s0 = 0, a0 = 0, ns = NS[0], na = NA[0];
#pragma unroll
      for (unsigned i = 1; i < 4u; ++i) if (x >= i) { s0 += NS[i - 1]; a0 += NA[i - 1]; ns = NS[i]; na = NA[i]; }
      const unsigned iS = s0 + k, iA = a0 + k - ns, iR = slot - s0 - a0 - ns - na;
      slot = k < ns ? (iS >> 3) * 32u + (iS & 7u) : (k < ns + na ? (iA >> 2) * 32u + 28u + (iA & 3u) : (iR / 20u) * 32u + 8u + iR % 20u);
    }
    sl[0] = (int)slot; sl[1] = (int)tg;
  }
  __syncthreads();
  const int slot = __builtin_amdgcn_readfirstlane(sl[0]); const unsigned tag = (unsigned)__builtin_amdgcn_readfirstlane(sl[1]);
  VMW(0);
  __syncthreads();
  const int xq = (slot >> 5) & 3, kq = slot & 31, wave = __builtin_amdgcn_readfirstlane(threadIdx.x >> 6);
#define ENTER __builtin_amdgcn_s_waitcnt(0)
  { const int fq = kq - (slot < 128 ? 8 : 12); if (fq >= 0 && fq < 20) f_part(a, shm, (slot >> 7) * 16 + 4 * xq + fq / 5, fq % 5, wave, slot < 128, tag); }
  if (slot < 128) {
    if (kq < 8) {
      const int h = xq * 8 + kq;
      if (wave == 0) { ENTER; s_role<0>(a, shm, h, tag); } else if (wave == 1) { ENTER; s_role<1>(a, shm, h, tag); } else if (wave == 2) { ENTER; s_role<2>(a, shm, h, tag); } else { ENTER; s_role<3>(a, shm, h, tag); }
    } else if (kq < 28) {
      const int u = xq * 20 + kq - 8;
      ENTER; r_role(a, shm, u, wave);
      if (u == 0 && wave == 0 && lane_id() == 0) __hip_atomic_store((gu32*)(a.ws + O_CNT) + W_EPOCH, tag, RLX_AGENT);
    } else {
      ENTER;
      { unsigned d0 = 0, d1 = 0; const unsigned vo = (unsigned)(((kq - 28) * 4 + wave) * 8192 + lane_id() * 128);
        asm volatile("global_load_dword %0, %2, %3\n global_load_dword %1, %4, %5\n s_waitcnt vmcnt(0)" : "+v"(d0), "+v"(d1) : "v"(vo), "s"(a.Bx), "v"(vo < 10240u ? vo : 0u), "s"(a.mem0)); }
      calibrate(a.ws, a.mem0, (xq * 4 + kq - 28) * 4 + wave, tag);
      if (kq & 1) {
        const char* src = (const char*)a.Bx + ((xq * 2 + ((kq - 28) >> 1)) * 4 + wave) * 8192 + lane_id() * 128;
#pragma unroll 1
        for (int i = 0; i < APB; ++i) touch(src + i * 262144, lds_addr(shm));
        VMW(0);
      }
      att_role(a, xq * 8 + (kq - 28) * 2 + (wave & 1), wave >> 1); }
  } else {
    if (kq < 12) { ENTER;
      { unsigned d0 = 0; asm volatile("global_load_dword %0, %1, %2\n s_waitcnt vmcnt(0)" : "+v"(d0) : "v"((unsigned)((kq * 4 + wave) * 8192 + lane_id() * 128)), "s"(a.Bx)); }
      g_role<true>(a, shm, (kq < 6 ? 0 : 18) + xq * 6 + kq, wave, tag); }
    else { ENTER; g_role<false>(a, shm, xq * 20 + kq - 12, wave, tag); }
    { ENTER; int w2 = wave, s2 = slot; asm volatile("" : "+s"(w2), "+s"(s2)); a_role(a, shm, s2 & 15, (s2 >> 4) & 7, w2); }
  }
}

extern "C" void kernel_launch(void* const* d_in, const int* in_sizes, int n_in, void* d_out, int out_size, void* d_ws, size_t ws_size, hipStream_t stream) {
  Args a;
  a.Ax = (const u16*)d_in[0]; a.Awqkv = (const u16*)d_in[1]; a.Awo = (const u16*)d_in[2]; a.Bx = (const u16*)d_in[3]; a.mem0 = (const u16*)d_in[4];
  a.Wq = (const u16*)d_in[5]; a.Wk = (const u16*)d_in[6]; a.Wv = (const u16*)d_in[7]; a.Wo = (const u16*)d_in[8];
  a.out = (u16*)d_out;
  a.ws = (char*)(((size_t)d_ws + 2 * MB - 1) & ~(2 * MB - 1));
  if ((size_t)(a.ws - (char*)d_ws) + O_END > ws_size) return;
  const int dd[8] = {DDV};
  for (int i = 0; i < 8; ++i) a.d[i] = dd[i];
  static_assert(CNT(C_TOTAL) * 4 == 15 * 4096, "");
  (void)hipFuncSetAttribute((const void*)k_mega, hipFuncAttributeMaxDynamicSharedMemorySize, 163840);
  k_mega<<<256, 256, 163840, stream>>>(a);
}
```
